```python
import torch

def _softmax(x, axis=-1):
    return torch.softmax(x, dim=axis)

def _gelu(x):
    return torch.nn.functional.gelu(x, approximate='tanh')

def _silu(x):
    return torch.nn.functional.silu(x)

def _sigmoid(x):
    return torch.sigmoid(x)

def _layernorm(x, axis=-1, eps=1e-05):
    m = x.mean(dim=axis, keepdim=True)
    v = x.var(dim=axis, keepdim=True, unbiased=False)
    return (x - m) / torch.sqrt(v + eps)

def _scatter_add(tgt, idx, src, axis=0):
    return tgt.clone().index_add_(axis, idx, src)

def _segment_sum(src, seg, n):
    out = torch.zeros((n,) + src.shape[1:], dtype=src.dtype)
    return out.index_add_(0, seg, src)

def _pad(x, pw):
    flat = tuple((v for pair in reversed(pw) for v in pair))
    return torch.nn.functional.pad(x, flat)
kernel_name = 'concat_heads__softmax_full_mha__memory_aug_recurrent_mem_tr__prompt'

def setup_inputs():

    _w = lambda a: (lambda t: t.to(torch.bfloat16) if t.is_floating_point() else t)(torch.as_tensor(a))

    def _A_setup():
        B, S, D = (1, 2048, 576)
        return {'x': _w((lambda *s: torch.randn(*s, dtype=torch.bfloat16) * 0.2)(B, S, D)), 'wqkv': _w((lambda *s: torch.randn(*s, dtype=torch.bfloat16) * 0.2)(D, 3 * D)), 'wo': _w((lambda *s: torch.randn(*s, dtype=torch.bfloat16) * 0.05)(D, D))}
    _A_in = _A_setup()

    def _B_setup():
        B, S, Dm = (1, 2048, 576)
        H, Dh = (32, 128)
        M, seg = (8, 32)
        seg = min(seg, S)
        S = S // seg * seg
        return {'x': _w((lambda *s: torch.randn(*s, dtype=torch.bfloat16) * 0.05)(B, S, Dm)), 'mem0': _w((lambda *s: torch.randn(*s, dtype=torch.bfloat16) * 0.8)(B, M, Dm)), 'Wq': _w((lambda *s: torch.randn(*s, dtype=torch.bfloat16) * 1.6)(Dm, H, Dh) * Dm ** (-0.5)), 'Wk': _w((lambda *s: torch.randn(*s, dtype=torch.bfloat16) * 1.6)(Dm, H, Dh) * Dm ** (-0.5)), 'Wv': _w((lambda *s: torch.randn(*s, dtype=torch.bfloat16) * 0.8)(Dm, H, Dh) * Dm ** (-0.5)), 'Wo': _w((lambda *s: torch.randn(*s, dtype=torch.bfloat16) * 0.8)(H, Dh, Dm) * (H * Dh) ** (-0.5)), 'seg_len': seg}
    _B_in = _B_setup()
    return {'A_x': _A_in['x'], 'A_wqkv': _A_in['wqkv'], 'A_wo': _A_in['wo'], 'B_x': _B_in['x'], 'B_mem0': _B_in['mem0'], 'B_Wq': _B_in['Wq'], 'B_Wk': _B_in['Wk'], 'B_Wv': _B_in['Wv'], 'B_Wo': _B_in['Wo'], 'B_seg_len': _B_in['seg_len']}

def reference(A_x, A_wqkv, A_wo, B_x, B_mem0, B_Wq, B_Wk, B_Wv, B_Wo, B_seg_len):

    def _A_ref(x, wqkv, wo):
        B, S, D = x.shape
        H = 32
        Dh = D // H
        qkv = (lambda x, *d: x.permute(*d))((x @ wqkv).reshape(B, S, 3, H, Dh), 2, 0, 3, 1, 4)
        q, k, v = (qkv[0], qkv[1], qkv[2])
        scale = Dh ** (-0.5)
        scores = torch.einsum('bhqd,bhkd->bhqk', q, k) * scale
        p = _softmax(scores, axis=-1)
        o = torch.einsum('bhqk,bhkd->bhqd', p, v)
        return (lambda x, *d: x.permute(*d))(o, 0, 2, 1, 3).reshape(B, S, D) @ wo

    def _B_ref(x, mem0, Wq, Wk, Wv, Wo, seg_len):
        B, S, Dm = x.shape
        H, Dh = (Wq.shape[1], Wq.shape[2])
        M = mem0.shape[1]
        scale = Dh ** (-0.5)
        n_seg = S // seg_len
        L = M + seg_len
        idx = torch.arange(L)
        mask = (idx[:, None] < M) | (idx[:, None] >= idx[None, :])
        mem = mem0
        outs = []
        for s in range(n_seg):
            xs = x[:, s * seg_len:(s + 1) * seg_len, :].clone()
            h = torch.concatenate([mem, xs], axis=1)
            q = torch.einsum('btd,dhe->bhte', h, Wq)
            k = torch.einsum('btd,dhe->bhte', h, Wk)
            v = torch.einsum('btd,dhe->bhte', h, Wv)
            scores = torch.einsum('bhqe,bhke->bhqk', q, k) * scale
            scores = torch.where(mask[None, None], scores, -1000000000.0)
            p = _softmax(scores, axis=-1)
            o = torch.einsum('bhqk,bhke->bhqe', p, v)
            o = torch.einsum('bhte,hed->btd', o, Wo)
            mem = o[:, :M, :].clone()
            outs.append(o[:, M:, :])
        return torch.concatenate(outs, axis=1)
    a = _A_ref(A_x, A_wqkv, A_wo)
    b = _B_ref(B_x, B_mem0, B_Wq, B_Wk, B_Wv, B_Wo, B_seg_len)
    return torch.concatenate([a, b], axis=-1)
```

```cpp
#include <hip/hip_runtime.h>
#include <hip/hip_bf16.h>

typedef __bf16 bf2 __attribute__((ext_vector_type(2)));
typedef __bf16 bf8 __attribute__((ext_vector_type(8)));
typedef __bf16 bf16x __attribute__((ext_vector_type(16)));
typedef short s4 __attribute__((ext_vector_type(4)));
typedef float f2 __attribute__((ext_vector_type(2)));
typedef float f4 __attribute__((ext_vector_type(4)));
typedef float f16v __attribute__((ext_vector_type(16)));
typedef unsigned u2 __attribute__((ext_vector_type(2)));
typedef unsigned u4 __attribute__((ext_vector_type(4)));
typedef unsigned u8v __attribute__((ext_vector_type(8)));
#define LDSP __attribute__((address_space(3)))
#define DI __device__ __forceinline__
typedef __amdgpu_buffer_rsrc_t rsrc_t;

constexpr unsigned F_UPROG = 0, F_KPROG = 128, F_ZPROG = 256, F_PPROG = 384, F_G1 = 512, F_APROG = 1024;
constexpr unsigned FLAG_BYTES = 2048;
constexpr unsigned M_SLOT = 9216, P_SLOT = 32 * 640, Z_SLOT = 36 * 32 * 256;
constexpr unsigned W_MRING = 4096;
constexpr unsigned W_PRING = W_MRING + 3 * M_SLOT;
constexpr unsigned W_ZRING = W_PRING + 2 * P_SLOT;
constexpr unsigned RING_END = W_ZRING + 2 * Z_SLOT;
constexpr unsigned W_KX = 1u << 20;
constexpr unsigned W_QX = W_KX + 64 * 32 * 8192;
constexpr unsigned W_U = W_QX + 64 * 32 * 8192;
constexpr unsigned W_ZA = W_U + 64 * 36 * 32 * 1024;
constexpr unsigned W_PP = W_ZA + 64 * Z_SLOT;
constexpr unsigned W_QKV = W_PP + 64 * 32 * 2560;
constexpr unsigned W_OA = W_QKV + 2048 * 1728 * 2;
constexpr unsigned W_DBG = W_OA + 2048 * 576 * 2;
constexpr unsigned W_END = W_DBG + (8u << 20);
static_assert(RING_END <= W_KX, "ring");
constexpr int LDS_BYTES = 155648 + 64;
constexpr int SC1 = 16;
constexpr unsigned SENT = 0xFFFFFFFFu;
constexpr unsigned SPIN_MAX = 1u << 17;

struct Args {
    const void* ax; const void* awqkv; const void* awo; const void* bx; const void* mem0;
    const void* wq; const void* wk; const void* wv; const void* wo; void* out; void* ws;
};

DI rsrc_t mk(const void* p, unsigned bytes) { return __builtin_amdgcn_make_buffer_rsrc((void*)p, 0, bytes, 0x00020000); }
template <int AUX> DI u4 ld16(rsrc_t r, unsigned off, unsigned so = 0) { return __builtin_amdgcn_raw_buffer_load_b128(r, off, so, AUX); }
template <int AUX> DI unsigned ld4(rsrc_t r, unsigned off, unsigned so = 0) { return __builtin_amdgcn_raw_buffer_load_b32(r, off, so, AUX); }
template <int AUX> DI void st16(u4 v, rsrc_t r, unsigned off, unsigned so = 0) { __builtin_amdgcn_raw_buffer_store_b128(v, r, off, so, AUX); }
template <int AUX> DI void st8(u2 v, rsrc_t r, unsigned off, unsigned so = 0) { __builtin_amdgcn_raw_buffer_store_b64(v, r, off, so, AUX); }
template <int AUX> DI void st4(unsigned v, rsrc_t r, unsigned off, unsigned so = 0) { __builtin_amdgcn_raw_buffer_store_b32(v, r, off, so, AUX); }
DI void bar() { asm volatile("s_waitcnt lgkmcnt(0)\n\ts_barrier" ::: "memory"); }
DI void wait_vm() { asm volatile("s_waitcnt vmcnt(0)" ::: "memory"); }
DI void cfence() { asm volatile("" ::: "memory"); }
DI unsigned pk(float a, float b) { f2 x = {a, b}; return __builtin_bit_cast(unsigned, __builtin_convertvector(x, bf2)); }
DI u4 lds16(const char* p) { return *(const LDSP u4*)p; }
DI u2 lds8(const char* p) { return *(const LDSP u2*)p; }
DI void sts16(char* p, u4 v) { *(LDSP u4*)p = v; }
DI void sts8(char* p, u2 v) { *(LDSP u2*)p = v; }
DI void sts4(char* p, unsigned v) { *(LDSP unsigned*)p = v; }
DI u2 ldtr(const char* p) { return __builtin_bit_cast(u2, __builtin_amdgcn_ds_read_tr16_b64_v4i16((LDSP s4*)p)); }
DI f4 mfma16(u4 a, u4 b, f4 c) { return __builtin_amdgcn_mfma_f32_16x16x32_bf16(__builtin_bit_cast(bf8, a), __builtin_bit_cast(bf8, b), c, 0, 0, 0); }
DI f4 smf(u4 a, u8v b, f4 c, int idx) { return __builtin_amdgcn_smfmac_f32_16x16x64_bf16(__builtin_bit_cast(bf8, a), __builtin_bit_cast(bf16x, b), c, idx, 0, 0); }
DI f16v mfma32(u4 a, u4 b, f16v c) { return __builtin_amdgcn_mfma_f32_32x32x16_bf16(__builtin_bit_cast(bf8, a), __builtin_bit_cast(bf8, b), c, 0, 0, 0); }
DI f16v mfma32k8(u2 a, u2 b, f16v c) { return __builtin_amdgcn_mfma_f32_32x32x8bf16_1k(__builtin_bit_cast(s4, a), __builtin_bit_cast(s4, b), c, 0, 0, 0); }
DI unsigned fu(float x) { return __builtin_bit_cast(unsigned, x); }
DI float uf(unsigned x) { return __builtin_bit_cast(float, x); }
DI float swap32_add(float a, float b) { auto r = __builtin_amdgcn_permlane32_swap(fu(a), fu(b), false, false); return uf(r[0]) + uf(r[1]); }
DI float xmax32(float x) { auto r = __builtin_amdgcn_permlane32_swap(fu(x), fu(x), false, false); return fmaxf(uf(r[0]), uf(r[1])); }
DI float xmax16(float x) { auto r = __builtin_amdgcn_permlane16_swap(fu(x), fu(x), false, false); return fmaxf(uf(r[0]), uf(r[1])); }
DI float xadd32(float x) { auto r = __builtin_amdgcn_permlane32_swap(fu(x), fu(x), false, false); return uf(r[0]) + uf(r[1]); }
DI float xadd16(float x) { auto r = __builtin_amdgcn_permlane16_swap(fu(x), fu(x), false, false); return uf(r[0]) + uf(r[1]); }
DI u4 widen16(u2 x, u2 y) {
    auto a = __builtin_amdgcn_permlane16_swap(x[0], y[0], false, false);
    auto b = __builtin_amdgcn_permlane16_swap(x[1], y[1], false, false);
    u4 r = {a[0], b[0], a[1], b[1]}; return r;
}
DI u4 canon(u4 v) { u4 r; for (int i = 0; i < 4; ++i) r[i] = min(v[i], 0xFFFFFFFEu); return r; }
DI bool valid16(u4 v) { return max(max(v[0], v[1]), max(v[2], v[3])) != SENT; }
DI bool wall(bool p) { return __builtin_amdgcn_ballot_w64(p) == __builtin_amdgcn_ballot_w64(true); }
DI float ex2(float x) { return __builtin_amdgcn_exp2f(x); }

DI void wait_prog(rsrc_t ws, unsigned off, int n, unsigned need, unsigned& have, bool& dead, int lane) {
    if (have >= need || dead) return;
    unsigned o = off + (unsigned)(lane < n ? lane : 0) * 4;
    for (unsigned sp = 0;; ++sp) {
        cfence();
        unsigned v = ld4<SC1>(ws, o);
        if (wall(v >= need)) {
            unsigned h = need;
            if (wall(v >= need + 4)) h = need + 4; else if (wall(v >= need + 1)) h = need + 1;
            have = h; break;
        }
        if (sp > SPIN_MAX) { dead = true; break; }
        __builtin_amdgcn_s_sleep(4);
    }
    cfence();
}
DI u4 poll16(rsrc_t ws, unsigned off, bool& dead) {
    u4 v;
    for (unsigned sp = 0;; ++sp) {
        cfence();
        v = ld16<SC1>(ws, off);
        if (wall(valid16(v)) || dead) break;
        if (sp > SPIN_MAX) { dead = true; break; }
        __builtin_amdgcn_s_sleep(1);
    }
    cfence();
    return v;
}

DI unsigned swzA(unsigned row) { return 2u * ((row & 1) | ((((row >> 1) ^ (row >> 2)) & 1) << 1) | (((row >> 3) & 1) << 2)); }
DI unsigned swzB(unsigned row) { return 2u * ((((row >> 1) ^ (row >> 2)) & 1) | (((row >> 3) & 1) << 1)); }
template <int KIND> DI void wload(u4 (&raw)[18], const void* w, int h, int tid) {
    if (KIND == 0) {
        rsrc_t r = mk(w, 576 * 4096 * 2);
        unsigned base = (unsigned)(tid >> 4) * 8192 + h * 256 + (tid & 15) * 16;
#pragma unroll
        for (int i = 0; i < 18; ++i) raw[i] = ld16<0>(r, base, i * 32 * 8192);
    } else {
        rsrc_t r = mk(w, 576 * 4096 * 2);
        unsigned base = (unsigned)h * 147456 + tid * 16;
#pragma unroll
        for (int i = 0; i < 18; ++i) raw[i] = ld16<0>(r, base, i * 8192);
    }
}
template <int KIND> DI void wstore(const u4 (&raw)[18], char* lds, int tid) {
    if (KIND == 0) {
        unsigned row = tid >> 4;
        char* p = lds + row * 256 + 16 * ((tid & 15) ^ swzA(row));
#pragma unroll
        for (int i = 0; i < 18; ++i) sts16(p + i * 32 * 256, raw[i]);
    } else {
#pragma unroll
        for (int i = 0; i < 18; ++i) {
            unsigned n = i * 512 + tid, row = n / 72, ch = n - row * 72;
            sts16(lds + row * 1152 + 16 * (ch ^ swzB(row)), raw[i]);
        }
    }
}
template <int KIND> DI const char* wpiece(const char* lds, unsigned row, unsigned ct, unsigned p) {
    unsigned ch = 2 * ct + (p >> 1);
    if (KIND == 0) return lds + row * 256 + 16 * (ch ^ swzA(row)) + 8 * (p & 1);
    return lds + row * 1152 + 16 * (ch ^ swzB(row)) + 8 * (p & 1);
}
template <int KIND> DI u8v wfrag_s(const char* lds, int j, int ct, int lane) {
    unsigned gb = lane >> 4, q = (lane >> 2) & 3, p = lane & 3;
    unsigned r0 = 16 * (gb >> 1) + 4 * (gb & 1) + (q & 1) + 8 * (q >> 1);
    u8v f;
#pragma unroll
    for (int u = 0; u < 4; ++u) {
        u2 v = ldtr(wpiece<KIND>(lds, r0 + 2 * (u & 1), ct, p) + (64 * j + 32 * (u >> 1)) * (KIND == 0 ? 256 : 1152));
        f[2 * u] = v[0]; f[2 * u + 1] = v[1];
    }
    return f;
}
template <int KIND> DI u4 wfrag_d(const char* lds, int j, int ct, int lane) {
    unsigned g = lane >> 4, q = (lane >> 2) & 3, p = lane & 3;
    u4 f;
#pragma unroll
    for (int u = 0; u < 2; ++u) {
        u2 v = ldtr(wpiece<KIND>(lds, 8 * g + 4 * u + q, ct, p) + (32 * j) * (KIND == 0 ? 256 : 1152));
        f[2 * u] = v[0]; f[2 * u + 1] = v[1];
    }
    return f;
}

#define TS(slot) do {} while (0)

DI void gather_M(rsrc_t ws, rsrc_t m0, char* mimg, int s, int wv, int lane, bool& dead) {
    if (s == 0) {
        unsigned o = (lane & 7) * 1152 + (lane >> 3) * 16;
        sts16(mimg + wv * 1024 + lane * 16, ld16<0>(m0, o + wv * 128));
        if (wv == 7) sts16(mimg + 8192 + lane * 16, ld16<0>(m0, o + 1024));
    } else {
        unsigned base = W_MRING + (s % 3) * M_SLOT + lane * 16;
        u4 v = poll16(ws, base + wv * 1024, dead);
        sts16(mimg + wv * 1024 + lane * 16, v);
        if (wv == 7) { v = poll16(ws, base + 8192, dead); sts16(mimg + 8192 + lane * 16, v); }
    }
    bar();
}

DI void role_A(const Args& a, char* lds, int h, int tid) {
    const int lane = tid & 63, wv = __builtin_amdgcn_readfirstlane(tid >> 6);
    rsrc_t ws = mk(a.ws, W_END), m0 = mk(a.mem0, 8 * 576 * 2);
    u8v wq[9], wk[9];
    {
        u4 r1[18], r2[18];
        wload<0>(r1, a.wq, h, tid); wload<0>(r2, a.wk, h, tid);
        wstore<0>(r1, lds, tid); bar();
#pragma unroll
        for (int j = 0; j < 9; ++j) wq[j] = wfrag_s<0>(lds, j, wv, lane);
        bar();
        wstore<0>(r2, lds, tid); bar();
#pragma unroll
        for (int j = 0; j < 9; ++j) wk[j] = wfrag_s<0>(lds, j, wv, lane);
        bar();
    }
    char* mimg = lds; char* qk = lds + 9216;
    const int idx = (lane & 8) ? 0xEEEE : 0x4444;
    const int g4 = lane >> 4, col = lane & 15;
    bool dead = false; unsigned khave = 0;
    u4 kx[2][4], qx[4];
    auto prefetch = [&](int s) {
        if (wv < 3) {
            wait_prog(ws, F_KPROG + h * 4, 1, s + 1, khave, dead, lane);
            unsigned b = (unsigned)(s * 32 + h) * 8192 + lane * 16;
#pragma unroll
            for (int i = 0; i < 8; ++i) kx[i >> 2][i & 3] = ld16<SC1>(ws, W_KX + b, i * 1024);
            unsigned bq = W_QX + b + (wv == 2 ? 4096 : 0);
#pragma unroll
            for (int i = 0; i < 4; ++i) qx[i] = ld16<SC1>(ws, bq, i * 1024);
        }
    };
    prefetch(0);
    const float cs = 0.08838834764831845f * 1.4426950408889634f;
    for (int s = 0; s < 64; ++s) {
        TS(0);
        gather_M(ws, m0, mimg, s, wv, lane, dead);
        TS(1);
        if (wv == 0 && s > 0) {
            if (lane < 40) { u4 sv = {SENT, SENT, SENT, SENT}; st16<SC1>(sv, ws, W_PRING + ((s - 1) & 1) * P_SLOT + h * 640 + lane * 16); }
        }
        if (wv == 3 && s > 0 && lane == 0) st4<SC1>(s, ws, F_PPROG + h * 4);
        f4 dq = {0, 0, 0, 0}, dk = {0, 0, 0, 0};
#pragma unroll
        for (int j = 0; j < 9; ++j) {
            u4 m = lds16(mimg + j * 1024 + lane * 16);
            dq = smf(m, wq[j], dq, idx); dk = smf(m, wk[j], dk, idx);
        }
        {
            float t0 = swap32_add(dq[0], dk[0]), t1 = swap32_add(dq[1], dk[1]), t2 = swap32_add(dq[2], dk[2]), t3 = swap32_add(dq[3], dk[3]);
            u2 v = {pk(t0, t1), pk(t2, t3)};
            sts8(qk + (wv * 16 + col) * 32 + g4 * 8, v);
        }
        if (wv == 0) wait_vm();
        bar();
        TS(2);
        if (wv < 3) {
            u4 F[4];
#pragma unroll
            for (int kk = 0; kk < 4; ++kk) {
                const char* p = qk + (32 * kk + 4 * g4 + ((lane >> 2) & 3)) * 32 + 8 * (lane & 3);
                u2 x = ldtr(p), y = ldtr(p + 512);
                F[kk] = u4{x[0], x[1], y[0], y[1]};
            }
            f4 t0 = {0, 0, 0, 0}, t1 = t0, tm = t0;
            if (wv == 0) {
#pragma unroll
                for (int kk = 0; kk < 4; ++kk) { t0 = mfma16(kx[0][kk], F[kk], t0); t1 = mfma16(kx[1][kk], F[kk], t1); tm = mfma16(F[kk], F[kk], tm); }
            } else {
#pragma unroll
                for (int kk = 0; kk < 4; ++kk) { t0 = mfma16(kx[0][kk], qx[kk], t0); t1 = mfma16(kx[1][kk], qx[kk], t1); tm = mfma16(F[kk], qx[kk], tm); }
            }
            const float NEG = -1.0e9f * 1.4426950408889634f;
            float v[12];
#pragma unroll
            for (int i = 0; i < 4; ++i) {
                v[i] = t0[i] * cs; v[4 + i] = t1[i] * cs; v[8 + i] = g4 >= 2 ? tm[i] * cs : -INFINITY;
                bool tri = (4 * g4 + i) <= col;
                if (wv == 1) { v[i] = tri ? v[i] : NEG; v[4 + i] = NEG; }
                if (wv == 2) { v[4 + i] = tri ? v[4 + i] : NEG; }
            }
            float m = v[0];
#pragma unroll
            for (int i = 1; i < 12; ++i) m = fmaxf(m, v[i]);
            m = xmax32(xmax16(m));
            float sum = 0;
#pragma unroll
            for (int i = 0; i < 12; ++i) { v[i] = ex2(v[i] - m); sum += v[i]; }
            sum = xadd32(xadd16(sum));
            float r = __builtin_amdgcn_rcpf(sum);
            u4 px = {pk(v[0] * r, v[1] * r), pk(v[2] * r, v[3] * r), pk(v[4] * r, v[5] * r), pk(v[6] * r, v[7] * r)};
            u2 pm2 = {pk(v[8] * r, v[9] * r), pk(v[10] * r, v[11] * r)};
            u4 pm = widen16(pm2, pm2);
            if (wv == 0) {
                if (s < 63) {
                    unsigned b = W_PRING + (s & 1) * P_SLOT + h * 640;
                    if (col < 8) st16<SC1>(canon(px), ws, b + g4 * 128 + col * 16);
                    if (col < 8 && g4 == 2) st16<SC1>(canon(pm), ws, b + 512 + col * 16);
                }
            } else {
                unsigned b = W_PP + (unsigned)(s * 32 + h) * 2560;
                st16<SC1>(px, ws, b + (wv - 1) * 1024 + lane * 16);
                if (g4 == 2) st16<SC1>(pm, ws, b + 2048 + (wv - 1) * 256 + col * 16);
                wait_vm();
            }
            TS(3);
            if (s < 63) prefetch(s + 1);
        }
    }
    bar();
    if (tid == 0) st4<SC1>(64, ws, F_PPROG + h * 4);
}

DI void role_V(const Args& a, char* lds, int h, int tid) {
    const int lane = tid & 63, wv = __builtin_amdgcn_readfirstlane(tid >> 6);
    rsrc_t ws = mk(a.ws, W_END), m0 = mk(a.mem0, 8 * 576 * 2);
    u8v wvv[9], wo[5][2];
    {
        u4 r1[18], r2[18];
        wload<0>(r1, a.wv, h, tid); wload<1>(r2, a.wo, h, tid);
        wstore<0>(r1, lds, tid); bar();
#pragma unroll
        for (int j = 0; j < 9; ++j) wvv[j] = wfrag_s<0>(lds, j, wv, lane);
        bar();
        wstore<1>(r2, lds, tid); bar();
#pragma unroll
        for (int i = 0; i < 5; ++i)
#pragma unroll
            for (int kk = 0; kk < 2; ++kk) wo[i][kk] = wfrag_s<1>(lds, kk, i < 4 ? wv + 8 * i : 32 + (wv & 3), lane);
        bar();
    }
    char* mimg = lds; char* vimg = lds + 9216;
    const int idx = (lane & 8) ? 0xEEEE : 0x4444;
    const int g4 = lane >> 4, col = lane & 15;
    const unsigned o4 = (unsigned)((wv + 8 * (((g4 & 1) << 1) | (g4 >> 1))) * 32 + h) * 256 + col * 16;
    const unsigned o5 = (unsigned)((32 + (wv & 3)) * 32 + h) * 256 + col * 16;
    bool dead = false;
    for (int s = 0; s < 64; ++s) {
        gather_M(ws, m0, mimg, s, wv, lane, dead);
        TS(1);
        if (s > 0) {
            u4 sv = {SENT, SENT, SENT, SENT};
            unsigned b = W_ZRING + ((s - 1) & 1) * Z_SLOT;
            st16<SC1>(sv, ws, b + o4);
            if (wv < 4 && g4 == 0) st16<SC1>(sv, ws, b + o5);
            if (tid == 448) st4<SC1>(s, ws, F_ZPROG + h * 4);
        }
        f4 d = {0, 0, 0, 0};
#pragma unroll
        for (int j = 0; j < 9; ++j) d = smf(lds16(mimg + j * 1024 + lane * 16), wvv[j], d, idx);
        sts4(vimg + (wv * 16 + col) * 16 + (g4 & 1) * 8 + (g4 >> 1) * 4, pk(swap32_add(d[0], d[2]), swap32_add(d[1], d[3])));
        bar();
        u4 av[2];
#pragma unroll
        for (int kk = 0; kk < 2; ++kk) {
            const char* p = vimg + (64 * kk + 16 * g4 + 8 * ((lane >> 1) & 1) + ((lane >> 2) & 3)) * 16 + 8 * (lane & 1);
            u2 x = ldtr(p), y = ldtr(p + 64);
            av[kk] = u4{x[0], x[1], y[0], y[1]};
        }
        f4 z[5];
#pragma unroll
        for (int i = 0; i < 4; ++i) { z[i] = f4{0, 0, 0, 0}; z[i] = smf(av[0], wo[i][0], z[i], idx); z[i] = smf(av[1], wo[i][1], z[i], idx); }
        z[4] = f4{0, 0, 0, 0};
        if (wv < 4) { z[4] = smf(av[0], wo[4][0], z[4], idx); z[4] = smf(av[1], wo[4][1], z[4], idx); }
        u2 X = {pk(swap32_add(z[0][0], z[1][0]), swap32_add(z[0][1], z[1][1])), pk(swap32_add(z[0][2], z[1][2]), swap32_add(z[0][3], z[1][3]))};
        u2 Y = {pk(swap32_add(z[2][0], z[3][0]), swap32_add(z[2][1], z[3][1])), pk(swap32_add(z[2][2], z[3][2]), swap32_add(z[2][3], z[3][3]))};
        u4 o = canon(widen16(X, Y));
        wait_vm();
        unsigned b = W_ZRING + (s & 1) * Z_SLOT;
        if (s < 63) st16<SC1>(o, ws, b + o4);
        u4 o5v;
        if (wv < 4) {
            u2 Z = {pk(xadd32(z[4][0]), xadd32(z[4][1])), pk(xadd32(z[4][2]), xadd32(z[4][3]))};
            o5v = canon(widen16(Z, Z));
            if (s < 63 && g4 == 0) st16<SC1>(o5v, ws, b + o5);
        }
        TS(3);
        unsigned ba = W_ZA + (unsigned)s * Z_SLOT;
        st16<SC1>(o, ws, ba + o4);
        if (wv < 4 && g4 == 0) st16<SC1>(o5v, ws, ba + o5);
        wait_vm();
    }
    bar();
    if (tid == 0) st4<SC1>(64, ws, F_ZPROG + h * 4);
}

DI void role_R(const Args& a, char* lds, int T, int tid) {
    const int lane = tid & 63, wv = __builtin_amdgcn_readfirstlane(tid >> 6);
    rsrc_t ws = mk(a.ws, W_END);
    const int g4 = lane >> 4, half = (lane >> 3) & 1, r = lane & 7;
    bool dead = false; unsigned uhave = 0;
    u4 uf_[4];
    auto prefetch = [&](int s) {
        wait_prog(ws, F_UPROG + wv * 16, 4, s + 1, uhave, dead, lane);
        unsigned b = W_U + (unsigned)((s * 36 + T) * 32 + 4 * wv) * 1024 + lane * 16;
#pragma unroll
        for (int i = 0; i < 4; ++i) uf_[i] = ld16<SC1>(ws, b, i * 1024);
    };
    prefetch(0);
    const unsigned po0 = (unsigned)(4 * wv + half) * 640 + g4 * 128 + r * 16;
    const unsigned pom = (unsigned)(4 * wv + g4) * 640 + 512 + r * 16;
    const unsigned zo = (unsigned)(T * 32 + 4 * wv) * 256 + lane * 16;
    for (int s = 0; s < 63; ++s) {
        TS(0);
        u4 z = poll16(ws, W_ZRING + (s & 1) * Z_SLOT + zo, dead);
        TS(1);
        unsigned pb = W_PRING + (s & 1) * P_SLOT;
        u4 p0, p1, pm;
        for (unsigned sp = 0;; ++sp) {
            cfence();
            p0 = ld16<SC1>(ws, pb + po0); p1 = ld16<SC1>(ws, pb + po0 + 1280); pm = ld16<SC1>(ws, pb + pom);
            if (wall(valid16(p0) && valid16(p1) && valid16(pm)) || dead) break;
            if (sp > SPIN_MAX) { dead = true; break; }
            __builtin_amdgcn_s_sleep(1);
        }
        cfence();
        TS(2);
        f4 a0 = {0, 0, 0, 0}, a1 = a0;
        a0 = mfma16(uf_[0], p0, a0); a1 = mfma16(uf_[1], p0, a1);
        a0 = mfma16(uf_[2], p1, a0); a1 = mfma16(uf_[3], p1, a1);
        a0 = mfma16(z, pm, a0);
        f4 sel;
#pragma unroll
        for (int i = 0; i < 4; ++i) {
            float o = uf(__builtin_amdgcn_update_dpp(0u, fu(a1[i]), 0x128, 0xf, 0xf, false));
            sel[i] = a0[i] + o;
        }
        char* red = lds + (s & 1) * 4096;
        if (half == 0) sts16(red + wv * 512 + r * 64 + g4 * 16, __builtin_bit_cast(u4, sel));
        bar();
        if (wv == 0) {
            const char* p = red + (lane >> 5) * 2048 + r * 64 + (2 * half + (g4 & 1)) * 16;
            f4 t = __builtin_bit_cast(f4, lds16(p));
#pragma unroll
            for (int i = 1; i < 4; ++i) t += __builtin_bit_cast(f4, lds16(p + i * 512));
            u2 w2 = {pk(xadd32(t[0]), xadd32(t[1])), pk(xadd32(t[2]), xadd32(t[3]))};
            u4 o = canon(widen16(w2, w2));
            if (lane < 16) {
                st16<SC1>(o, ws, W_MRING + ((s + 1) % 3) * M_SLOT + T * 256 + lane * 16);
                u4 sv = {SENT, SENT, SENT, SENT};
                st16<SC1>(sv, ws, W_MRING + (s % 3) * M_SLOT + T * 256 + lane * 16);
            }
            TS(3);
            wait_vm();
        }
        if (s < 62) prefetch(s + 1);
    }
}

DI void xload(u4 (&xr)[5], rsrc_t xs, int seg, int wv, int lane) {
    unsigned o = (unsigned)(seg * 32 + (lane & 15)) * 1152 + (lane >> 4) * 16;
#pragma unroll
    for (int i = 0; i < 5; ++i) {
        int f = i < 4 ? wv + 8 * i : 32 + (wv & 3);
        int rt = f >= 18, j = f - 18 * rt;
        xr[i] = ld16<0>(xs, o, rt * 16 * 1152 + j * 64);
    }
}
DI void xstore(const u4 (&xr)[5], char* xb, int wv, int lane) {
#pragma unroll
    for (int i = 0; i < 5; ++i) {
        int f = i < 4 ? wv + 8 * i : 32 + (wv & 3);
        sts16(xb + f * 1024 + lane * 16, xr[i]);
    }
}

DI void role_QK(const Args& a, char* lds, int h, int tid) {
    const int lane = tid & 63, wv = __builtin_amdgcn_readfirstlane(tid >> 6);
    rsrc_t ws = mk(a.ws, W_END), xs = mk(a.bx, 2048 * 576 * 2);
    u4 w[2][18];
    u4 xr[5];
    {
        u4 r1[18], r2[18];
        wload<0>(r1, a.wk, h, tid); wload<0>(r2, a.wq, h, tid);
        wstore<0>(r1, lds, tid); bar();
        if (wv >= 4) {
#pragma unroll
            for (int j = 0; j < 18; ++j) { w[0][j] = wfrag_d<0>(lds, j, 2 * (wv & 3), lane); w[1][j] = wfrag_d<0>(lds, j, 2 * (wv & 3) + 1, lane); }
        }
        bar();
        wstore<0>(r2, lds, tid); bar();
        xload(xr, xs, 0, wv, lane);
        if (wv < 4) {
#pragma unroll
            for (int j = 0; j < 18; ++j) { w[0][j] = wfrag_d<0>(lds, j, 2 * (wv & 3), lane); w[1][j] = wfrag_d<0>(lds, j, 2 * (wv & 3) + 1, lane); }
        }
        bar();
    }
    xstore(xr, lds, wv, lane);
    bar();
    const unsigned ob = (wv < 4 ? W_QX : W_KX) + (unsigned)h * 8192 + (wv & 3) * 1024 + lane * 16;
    for (int s = 0; s < 64; ++s) {
        const char* xb = lds + (s & 1) * 36864;
        if (s < 63) xload(xr, xs, s + 1, wv, lane);
        f4 c[2][2] = {};
#pragma unroll
        for (int j = 0; j < 18; ++j) {
            u4 x0 = lds16(xb + j * 1024 + lane * 16), x1 = lds16(xb + (18 + j) * 1024 + lane * 16);
            c[0][0] = mfma16(w[0][j], x0, c[0][0]); c[0][1] = mfma16(w[1][j], x0, c[0][1]);
            c[1][0] = mfma16(w[0][j], x1, c[1][0]); c[1][1] = mfma16(w[1][j], x1, c[1][1]);
        }
        wait_vm();
        if (s < 63) xstore(xr, lds + ((s + 1) & 1) * 36864, wv, lane);
#pragma unroll
        for (int rt = 0; rt < 2; ++rt) {
            u4 o = {pk(c[rt][0][0], c[rt][0][1]), pk(c[rt][0][2], c[rt][0][3]), pk(c[rt][1][0], c[rt][1][1]), pk(c[rt][1][2], c[rt][1][3])};
            st16<SC1>(o, ws, ob + rt * 4096, (unsigned)s * 32 * 8192);
        }
        if (s < 2 || s == 63) wait_vm();
        bar();
        if (tid == 0 && s != 2) st4<SC1>((s < 2 || s == 63) ? s + 1 : s, ws, F_KPROG + h * 4);
    }
}

DI void role_U(const Args& a, char* lds, int h, int tid) {
    const int lane = tid & 63, wv = __builtin_amdgcn_readfirstlane(tid >> 6);
    rsrc_t ws = mk(a.ws, W_END), xs = mk(a.bx, 2048 * 576 * 2);
    u4 w[18], wo[5][4];
    u4 xr[5];
    {
        u4 r1[18], r2[18];
        wload<0>(r1, a.wv, h, tid); wload<1>(r2, a.wo, h, tid);
        wstore<0>(r1, lds, tid); bar();
#pragma unroll
        for (int j = 0; j < 18; ++j) w[j] = wfrag_d<0>(lds, j, wv, lane);
        bar();
        wstore<1>(r2, lds, tid); bar();
        xload(xr, xs, 0, wv, lane);
#pragma unroll
        for (int i = 0; i < 5; ++i)
#pragma unroll
            for (int kk = 0; kk < 4; ++kk) wo[i][kk] = wfrag_d<1>(lds, kk, i < 4 ? wv + 8 * i : 32 + (wv & 3), lane);
        bar();
    }
    xstore(xr, lds, wv, lane);
    bar();
    const int g4 = lane >> 4, col = lane & 15;
    const unsigned vo = (unsigned)(wv >> 1) * 1024 + ((2 * (wv & 1) + (g4 >> 1)) * 16 + col) * 16 + (g4 & 1) * 8;
    for (int s = 0; s < 64; ++s) {
        const char* xb = lds + (s & 1) * 36864;
        char* vb = lds + 73728 + (s & 1) * 8192;
        if (s < 63) xload(xr, xs, s + 1, wv, lane);
        f4 c0 = {0, 0, 0, 0}, c1 = c0;
#pragma unroll
        for (int j = 0; j < 18; ++j) {
            c0 = mfma16(w[j], lds16(xb + j * 1024 + lane * 16), c0);
            c1 = mfma16(w[j], lds16(xb + (18 + j) * 1024 + lane * 16), c1);
        }
        sts8(vb + vo, u2{pk(c0[0], c0[1]), pk(c0[2], c0[3])});
        sts8(vb + 4096 + vo, u2{pk(c1[0], c1[1]), pk(c1[2], c1[3])});
        wait_vm();
        if (s < 63) xstore(xr, lds + ((s + 1) & 1) * 36864, wv, lane);
        bar();
        if (tid == 0 && s > 2) st4<SC1>(s, ws, F_UPROG + h * 4);
        u4 va[2][4];
#pragma unroll
        for (int i = 0; i < 8; ++i) va[i >> 2][i & 3] = lds16(vb + i * 1024 + lane * 16);
#pragma unroll
        for (int i = 0; i < 5; ++i) {
            if (i < 4 || wv < 4) {
                f4 d0 = {0, 0, 0, 0}, d1 = d0;
#pragma unroll
                for (int kk = 0; kk < 4; ++kk) { d0 = mfma16(va[0][kk], wo[i][kk], d0); d1 = mfma16(va[1][kk], wo[i][kk], d1); }
                u4 o = {pk(d0[0], d0[1]), pk(d0[2], d0[3]), pk(d1[0], d1[1]), pk(d1[2], d1[3])};
                int T = i < 4 ? wv + 8 * i : 32 + wv;
                st16<SC1>(o, ws, W_U + (unsigned)h * 1024 + lane * 16, (unsigned)(s * 36 + T) * 32768);
            }
        }
        if (s < 2 || s == 63) {
            wait_vm(); bar();
            if (tid == 0) st4<SC1>(s + 1, ws, F_UPROG + h * 4);
        }
    }
}

DI void role_XO(const Args& a, char* lds, int T0, int par, int tid) {
    const int lane = tid & 63, wv = __builtin_amdgcn_readfirstlane(tid >> 6);
    rsrc_t ws = mk(a.ws, W_END), ro = mk(a.out, 2048 * 1152 * 2);
    const int g4 = lane >> 4, col = lane & 15;
    bool dead = false; unsigned uhave = 0, zhave = 0, phave = 0;
    for (int s = par; s < 64; s += 2) {
        u4 u[3][4], z[3], p[4][2], pm[2];
        wait_prog(ws, F_UPROG + wv * 16, 4, s + 1, uhave, dead, lane);
#pragma unroll
        for (int t = 0; t < 3; ++t)
#pragma unroll
            for (int i = 0; i < 4; ++i) u[t][i] = ld16<SC1>(ws, W_U + (unsigned)(4 * wv + i) * 1024 + lane * 16, (unsigned)(s * 36 + T0 + t) * 32768);
        wait_prog(ws, F_ZPROG + wv * 16, 4, s + 1, zhave, dead, lane);
#pragma unroll
        for (int t = 0; t < 3; ++t) z[t] = ld16<SC1>(ws, W_ZA + (unsigned)(4 * wv) * 256 + lane * 16, (unsigned)(s * 36 + T0 + t) * 8192);
        wait_prog(ws, F_PPROG + wv * 16, 4, s + 1, phave, dead, lane);
        unsigned pb = W_PP + (unsigned)(s * 32 + 4 * wv) * 2560;
#pragma unroll
        for (int i = 0; i < 4; ++i)
#pragma unroll
            for (int rt = 0; rt < 2; ++rt) p[i][rt] = ld16<SC1>(ws, pb + lane * 16, i * 2560 + rt * 1024);
#pragma unroll
        for (int rt = 0; rt < 2; ++rt) pm[rt] = ld16<SC1>(ws, pb + g4 * 2560 + 2048 + col * 16, rt * 256);
        char* red = lds + ((s >> 1) & 1) * 49152;
#pragma unroll
        for (int t = 0; t < 3; ++t)
#pragma unroll
            for (int rt = 0; rt < 2; ++rt) {
                f4 c = {0, 0, 0, 0};
#pragma unroll
                for (int i = 0; i < 4; ++i) c = mfma16(u[t][i], p[i][rt], c);
                c = mfma16(z[t], pm[rt], c);
                sts16(red + (wv * 6 + t * 2 + rt) * 1024 + lane * 16, __builtin_bit_cast(u4, c));
            }
        bar();
        if (wv < 6) {
            f4 c = __builtin_bit_cast(f4, lds16(red + wv * 1024 + lane * 16));
#pragma unroll
            for (int i = 1; i < 8; ++i) c += __builtin_bit_cast(f4, lds16(red + (i * 6 + wv) * 1024 + lane * 16));
            unsigned o = (unsigned)(s * 32 + (wv & 1) * 16 + col) * 2304 + (576 + 16 * (T0 + (wv >> 1)) + 4 * g4) * 2;
            st8<0>(u2{pk(c[0], c[1]), pk(c[2], c[3])}, ro, o);
        }
    }
}

DI unsigned swzG(unsigned row) { return 2u * (((row >> 1) & 1) | (((row >> 3) & 1) << 1)); }
template <int XAUX, int OAUX>
DI void gemm_item(rsrc_t xr_, unsigned xoff, rsrc_t wr, unsigned ldw, rsrc_t orr, unsigned ooff, unsigned ldo, int row0, int col0, int npass, char* lds, int tid) {
    const int lane = tid & 63, wv = __builtin_amdgcn_readfirstlane(tid >> 6);
    const int g4 = lane >> 4, col = lane & 15;
    const unsigned wsrc = (unsigned)(tid >> 3) * ldw + (tid & 7) * 16 + col0 * 2;
    const unsigned wdst = (unsigned)(tid >> 3) * 128 + 16 * ((tid & 7) ^ swzG(tid >> 3));
    bar();
    {
        u4 t[9];
#pragma unroll
        for (int i = 0; i < 9; ++i) t[i] = ld16<0>(wr, wsrc, i * 64 * ldw);
#pragma unroll
        for (int i = 0; i < 9; ++i) sts16(lds + wdst + i * 8192, t[i]);
    }
    u4 x[2][18];
    {
        unsigned o = xoff + (unsigned)(row0 + wv * 32 + col) * 1152 + g4 * 16;
#pragma unroll
        for (int j = 0; j < 18; ++j) { x[0][j] = ld16<XAUX>(xr_, o, j * 64); x[1][j] = ld16<XAUX>(xr_, o, 16 * 1152 + j * 64); }
    }
    bar();
    const unsigned q = (lane >> 2) & 3, p = lane & 3;
    unsigned ra[2], rbs[2];
#pragma unroll
    for (int u = 0; u < 2; ++u) { ra[u] = (8 * g4 + 4 * u + q) * 128 + 8 * (p & 1); rbs[u] = swzG(8 * g4 + 4 * u + q); }
    for (int ps = 0; ps < npass; ++ps) {
        const char* wb = lds + (ps & 1) * 73728;
        char* wn = lds + ((ps + 1) & 1) * 73728;
        const bool more = ps + 1 < npass;
        f4 c[2][4] = {};
        u4 t[3];
#pragma unroll
        for (int j = 0; j < 18; ++j) {
            if (j % 6 == 0 && more) {
#pragma unroll
                for (int i = 0; i < 3; ++i) t[i] = ld16<0>(wr, wsrc + (ps + 1) * 128, ((j / 6) * 3 + i) * 64 * ldw);
            }
#pragma unroll
            for (int ct = 0; ct < 4; ++ct) {
                u2 w0 = ldtr(wb + j * 4096 + ra[0] + 16 * ((2 * ct + (p >> 1)) ^ rbs[0]));
                u2 w1 = ldtr(wb + j * 4096 + ra[1] + 16 * ((2 * ct + (p >> 1)) ^ rbs[1]));
                u4 wf = {w0[0], w0[1], w1[0], w1[1]};
                c[0][ct] = mfma16(wf, x[0][j], c[0][ct]); c[1][ct] = mfma16(wf, x[1][j], c[1][ct]);
            }
            if (j % 6 == 5 && more) {
#pragma unroll
                for (int i = 0; i < 3; ++i) sts16(wn + wdst + ((j / 6) * 3 + i) * 8192, t[i]);
            }
        }
#pragma unroll
        for (int rt = 0; rt < 2; ++rt)
#pragma unroll
            for (int cp = 0; cp < 2; ++cp) {
                u2 X = {pk(c[rt][2 * cp][0], c[rt][2 * cp][1]), pk(c[rt][2 * cp][2], c[rt][2 * cp][3])};
                u2 Y = {pk(c[rt][2 * cp + 1][0], c[rt][2 * cp + 1][1]), pk(c[rt][2 * cp + 1][2], c[rt][2 * cp + 1][3])};
                u4 o = widen16(X, Y);
                unsigned oo = ooff + (unsigned)(row0 + wv * 32 + rt * 16 + col) * ldo + (col0 + ps * 64 + cp * 32 + (g4 & 1) * 16 + (g4 >> 1) * 8) * 2;
                st16<OAUX>(o, orr, oo);
            }
        bar();
    }
}
DI void qkv_item(const Args& a, char* lds, int it, int tid) {
    rsrc_t ws = mk(a.ws, W_END);
    gemm_item<0, SC1>(mk(a.ax, 2048 * 576 * 2), 0, mk(a.awqkv, 576 * 1728 * 2), 3456, ws, W_QKV, 3456, (it / 9) * 256, (it % 9) * 192, 3, lds, tid);
    wait_vm(); bar();
    if (tid == 0) st4<SC1>(1, ws, F_G1 + it * 4);
}
DI void oproj_item(const Args& a, char* lds, int rb, int pass, int tid) {
    rsrc_t ws = mk(a.ws, W_END);
    bool dead = false; unsigned have = 0;
    wait_prog(ws, F_APROG + (rb >> 2) * 128, 32, (rb & 3) + 1, have, dead, tid & 63);
    gemm_item<SC1, 0>(ws, W_OA, mk(a.awo, 576 * 576 * 2), 1152, mk(a.out, 2048 * 1152 * 2), 0, 2304, rb * 256, pass * 64, 1, lds, tid);
}

DI void attn_role(const Args& a, char* lds, int h, int hf, int tid) {
    const int lane = tid & 63, wv = __builtin_amdgcn_readfirstlane(tid >> 6);
    rsrc_t ws = mk(a.ws, W_END);
    bool dead = false; unsigned h1 = 0, h2 = 0;
    wait_prog(ws, F_G1, 64, 1, h1, dead, lane);
    wait_prog(ws, F_G1 + 256, 8, 1, h2, dead, lane);
    char* km = lds; char* ke = lds + 65536; char* vi = lds + 65536 + 8192;
    bar();
#pragma unroll
    for (int i = 0; i < 4; ++i) {
        int key = i * 512 + tid;
        unsigned o = W_QKV + (unsigned)key * 3456 + 1152 + h * 36;
        u4 k0 = ld16<SC1>(ws, o), k1 = ld16<SC1>(ws, o + 16); unsigned k2 = ld4<SC1>(ws, o + 32);
        u4 v0 = ld16<SC1>(ws, o + 1152), v1 = ld16<SC1>(ws, o + 1168); unsigned v2 = ld4<SC1>(ws, o + 1184);
        sts16(km + (key >> 5) * 1024 + (key & 31) * 16, k0);
        sts16(km + (key >> 5) * 1024 + 512 + (key & 31) * 16, k1);
        sts4(ke + (key >> 6) * 256 + (key & 31) * 8 + ((key >> 5) & 1) * 4, k2);
        char* vp = vi + key * 40;
        sts8(vp, u2{v0[0], v0[1]}); sts8(vp + 8, u2{v0[2], v0[3]}); sts8(vp + 16, u2{v1[0], v1[1]}); sts8(vp + 24, u2{v1[2], v1[3]});
        sts8(vp + 32, u2{v2, 0x00003F80u});
    }
    bar();
    const int hh = lane >> 5, c32 = lane & 31;
    const float cs = 0.23570226039551584f * 1.4426950408889634f;
    const char* kp = km + lane * 16;
    const char* kep = ke + c32 * 8;
    const char* vp = vi + (4 * hh + ((lane >> 2) & 3)) * 40 + (((lane >> 4) & 1) ? 32 : 8 * (lane & 3));
    for (int ps = 0; ps < 4; ++ps) {
        int row = hf * 1024 + ps * 256 + wv * 32 + c32;
        unsigned qo = W_QKV + (unsigned)row * 3456 + h * 36;
        u4 q0 = ld16<SC1>(ws, qo + hh * 16);
        unsigned qe = ld4<SC1>(ws, qo + 32);
        if (hh) qe = 0;
        u2 qa = {qe, 0}, qb = {0, qe};
        f16v o = {};
        float m = -INFINITY;
        for (int kb = 0; kb < 16; ++kb) {
            f16v sc[4];
#pragma unroll
            for (int t = 0; t < 4; ++t) {
                u2 e = lds8(kep + (kb * 2 + (t >> 1)) * 256);
                f16v z = {};
                z = mfma32(lds16(kp + (kb * 4 + t) * 1024), q0, z);
                sc[t] = mfma32k8(e, (t & 1) ? qb : qa, z);
            }
            float mx = m;
#pragma unroll
            for (int t = 0; t < 4; ++t)
#pragma unroll
                for (int i = 0; i < 16; ++i) mx = fmaxf(mx, sc[t][i]);
            mx = xmax32(mx);
            float al = ex2((m - mx) * cs);
            m = mx;
            float mc = mx * cs;
#pragma unroll
            for (int i = 0; i < 12; ++i) o[i] *= al;
#pragma unroll
            for (int t = 0; t < 4; ++t) {
#pragma unroll
                for (int s2 = 0; s2 < 2; ++s2) {
                    u4 pf;
#pragma unroll
                    for (int i = 0; i < 4; ++i) pf[i] = pk(ex2(__builtin_fmaf(sc[t][8 * s2 + 2 * i], cs, -mc)), ex2(__builtin_fmaf(sc[t][8 * s2 + 2 * i + 1], cs, -mc)));
                    const char* v = vp + (kb * 128 + t * 32 + s2 * 16) * 40;
                    u2 x = ldtr(v), y = ldtr(v + 320);
                    o = mfma32(u4{x[0], x[1], y[0], y[1]}, pf, o);
                }
            }
        }
        float r = 1.0f / o[10];
        unsigned oo = W_OA + (unsigned)row * 1152 + h * 36 + hh * 8;
        st8<SC1>(u2{pk(o[0] * r, o[1] * r), pk(o[2] * r, o[3] * r)}, ws, oo);
        st8<SC1>(u2{pk(o[4] * r, o[5] * r), pk(o[6] * r, o[7] * r)}, ws, oo + 16);
        if (hh == 0) st4<SC1>(pk(o[8] * r, o[9] * r), ws, oo + 32);
        wait_vm(); bar();
        if (tid == 0) st4<SC1>(ps + 1, ws, F_APROG + hf * 128 + h * 4);
    }
}

__global__ void __launch_bounds__(512, 2) mega_kernel(Args a) {
    extern __shared__ __attribute__((aligned(16))) char lds[];
    const int tid = threadIdx.x, x = blockIdx.x & 7, sl = blockIdx.x >> 3;
    if (x < 4) {
        if (sl < 8) role_A(a, lds, x * 8 + sl, tid);
        else if (sl < 16) role_V(a, lds, x * 8 + sl - 8, tid);
        else if (sl < 25) role_R(a, lds, x * 9 + sl - 16, tid);
    } else {
        if (sl < 8) role_QK(a, lds, (x - 4) * 8 + sl, tid);
        else if (sl < 16) role_U(a, lds, (x - 4) * 8 + sl - 8, tid);
    }
    int qi = -1;
    if (x >= 4 && sl >= 16) qi = (x - 4) * 16 + sl - 16;
    if (x < 4 && sl == 31) qi = 64 + x;
    if (x < 4 && sl == 30) qi = 68 + x;
    if (qi >= 0) qkv_item(a, lds, qi, tid);
    if (x < 4 && sl >= 25 && sl < 31) role_XO(a, lds, x * 9 + 3 * ((sl - 25) >> 1), (sl - 25) & 1, tid);
    if (qi >= 0 && qi < 64) attn_role(a, lds, qi >> 1, qi & 1, tid);
    if (qi >= 0 && qi < 68) {
        int n = qi < 64 ? 1 : 2;
        for (int i = 0; i < n; ++i) oproj_item(a, lds, qi < 64 ? qi >> 3 : (qi - 64) + 4 * i, qi < 64 ? qi & 7 : 8, tid);
    }
}

extern "C" void kernel_launch(void* const* d_in, const int* in_sizes, int n_in, void* d_out, int out_size, void* d_ws, size_t ws_size,
                              hipStream_t stream) {
    (void)in_sizes; (void)n_in; (void)out_size; (void)ws_size;
    Args a;
    a.ax = d_in[0]; a.awqkv = d_in[1]; a.awo = d_in[2]; a.bx = d_in[3]; a.mem0 = d_in[4];
    a.wq = d_in[5]; a.wk = d_in[6]; a.wv = d_in[7]; a.wo = d_in[8]; a.out = d_out; a.ws = d_ws;
    (void)hipFuncSetAttribute((const void*)mega_kernel, hipFuncAttributeMaxDynamicSharedMemorySize, LDS_BYTES);
    (void)hipMemsetAsync(d_ws, 0, FLAG_BYTES, stream);
    (void)hipMemsetAsync((char*)d_ws + W_MRING, 0xFF, RING_END - W_MRING, stream);
    mega_kernel<<<256, 512, LDS_BYTES, stream>>>(a);
}
```

```cpp
#include <hip/hip_runtime.h>
#include <hip/hip_bf16.h>

typedef __bf16 bf2 __attribute__((ext_vector_type(2)));
typedef __bf16 bf8 __attribute__((ext_vector_type(8)));
typedef __bf16 bf16x __attribute__((ext_vector_type(16)));
typedef short s4 __attribute__((ext_vector_type(4)));
typedef float f2 __attribute__((ext_vector_type(2)));
typedef float f4 __attribute__((ext_vector_type(4)));
typedef float f16v __attribute__((ext_vector_type(16)));
typedef unsigned u2 __attribute__((ext_vector_type(2)));
typedef unsigned u4 __attribute__((ext_vector_type(4)));
typedef unsigned u8v __attribute__((ext_vector_type(8)));
#define LDSP __attribute__((address_space(3)))
#define DI __device__ __forceinline__
typedef __amdgpu_buffer_rsrc_t rsrc_t;

constexpr unsigned W_FLAG = 7u << 20, W_ARR = W_FLAG + 8192, W_EPOCH = W_FLAG + 12288;
constexpr unsigned F_UPROG = 0, F_KPROG = 32, F_ZPROG = 64, F_PPROG = 96, F_G1 = 128, F_APROG = 256;
constexpr unsigned F_RDYP = 384, F_RDYR = 448;
constexpr unsigned F_U0 = 520;
constexpr unsigned F_CPROG = 512;
constexpr unsigned MSL = 80u << 10, PSL = 160u << 10, ZSL = 576u << 10;
constexpr unsigned MB = 0, PB = 3 * MSL, ZB = 2u << 20, RING_BYTES = 4u << 20;
constexpr unsigned M_TS = 65536 + 4096, PROBE = (2u << 20) - 16384;
constexpr unsigned Z_SLOT = 36 * 32 * 256;
constexpr unsigned W_KX = 8u << 20;
constexpr unsigned W_QX = W_KX + 64 * 32 * 8192;
constexpr unsigned W_U = W_QX + 64 * 32 * 8192;
constexpr unsigned W_ZA = W_U + 64 * 36 * 32 * 1024;
constexpr unsigned W_PP = W_ZA + 64 * Z_SLOT;
constexpr unsigned W_QKV = W_PP + 64 * 32 * 2560;
constexpr unsigned W_OA = W_QKV + 2048 * 1728 * 2;
constexpr unsigned W_END = W_OA + 2048 * 576 * 2;
static_assert(PB + 3 * PSL <= PROBE && 3 * ZSL <= PROBE, "ring");
constexpr int LDS_BYTES = 155648 + 2048;
constexpr int SC1 = 16;
constexpr unsigned SENT = 0xFFFFFFFFu;
constexpr unsigned SPIN_MAX = 1u << 17;
#define UNL(x) __builtin_expect(!!(x), 0)

struct Args {
    const void* ax; const void* awqkv; const void* awo; const void* bx; const void* mem0;
    const void* wq; const void* wk; const void* wv; const void* wo; void* out; void* ws;
};

DI rsrc_t mk(const void* p, unsigned bytes) { return __builtin_amdgcn_make_buffer_rsrc((void*)p, 0, bytes, 0x00020000); }
template <int AUX> DI u4 ld16(rsrc_t r, unsigned off, unsigned so = 0) { return __builtin_amdgcn_raw_buffer_load_b128(r, off, so, AUX); }
template <int AUX> DI unsigned ld4(rsrc_t r, unsigned off, unsigned so = 0) { return __builtin_amdgcn_raw_buffer_load_b32(r, off, so, AUX); }
template <int AUX> DI void st16(u4 v, rsrc_t r, unsigned off, unsigned so = 0) { __builtin_amdgcn_raw_buffer_store_b128(v, r, off, so, AUX); }
template <int AUX> DI void st8(u2 v, rsrc_t r, unsigned off, unsigned so = 0) { __builtin_amdgcn_raw_buffer_store_b64(v, r, off, so, AUX); }
template <int AUX> DI void st4(unsigned v, rsrc_t r, unsigned off, unsigned so = 0) { __builtin_amdgcn_raw_buffer_store_b32(v, r, off, so, AUX); }
DI void bar() { asm volatile("s_waitcnt lgkmcnt(0)\n\ts_barrier" ::: "memory"); }
DI void wait_vm() { asm volatile("s_waitcnt vmcnt(0)" ::: "memory"); }
DI void cfence() { asm volatile("" ::: "memory"); }
DI unsigned pk(float a, float b) { f2 x = {a, b}; return __builtin_bit_cast(unsigned, __builtin_convertvector(x, bf2)); }
DI u4 lds16(const char* p) { return *(const LDSP u4*)p; }
DI u2 lds8(const char* p) { return *(const LDSP u2*)p; }
DI void sts16(char* p, u4 v) { *(LDSP u4*)p = v; }
DI void sts8(char* p, u2 v) { *(LDSP u2*)p = v; }
DI void sts4(char* p, unsigned v) { *(LDSP unsigned*)p = v; }
DI u2 ldtr(const char* p) { return __builtin_bit_cast(u2, __builtin_amdgcn_ds_read_tr16_b64_v4i16((LDSP s4*)p)); }
typedef const LDSP char* lp;
DI u4 lds16(lp p) { return *(const LDSP u4*)p; }
DI u2 lds8(lp p) { return *(const LDSP u2*)p; }
DI u2 ldtr(lp p) { return __builtin_bit_cast(u2, __builtin_amdgcn_ds_read_tr16_b64_v4i16((LDSP s4*)p)); }
DI f4 mfma16(u4 a, u4 b, f4 c) { return __builtin_amdgcn_mfma_f32_16x16x32_bf16(__builtin_bit_cast(bf8, a), __builtin_bit_cast(bf8, b), c, 0, 0, 0); }
DI f4 smf(u4 a, u8v b, f4 c, int idx) { return __builtin_amdgcn_smfmac_f32_16x16x64_bf16(__builtin_bit_cast(bf8, a), __builtin_bit_cast(bf16x, b), c, idx, 0, 0); }
DI f16v mfma32(u4 a, u4 b, f16v c) { return __builtin_amdgcn_mfma_f32_32x32x16_bf16(__builtin_bit_cast(bf8, a), __builtin_bit_cast(bf8, b), c, 0, 0, 0); }
DI f16v mfma32k8(u2 a, u2 b, f16v c) { return __builtin_amdgcn_mfma_f32_32x32x8bf16_1k(__builtin_bit_cast(s4, a), __builtin_bit_cast(s4, b), c, 0, 0, 0); }
DI unsigned fu(float x) { return __builtin_bit_cast(unsigned, x); }
DI float uf(unsigned x) { return __builtin_bit_cast(float, x); }
DI float swap32_add(float a, float b) { auto r = __builtin_amdgcn_permlane32_swap(fu(a), fu(b), false, false); return uf(r[0]) + uf(r[1]); }
DI float fmx(float a, float b) { return __builtin_fmaxf(a, b); }
DI float redmax5(float a, float b, float c, float d, float e) {
    float m, t;
    asm("v_max3_f32 %0, %2, %3, %4\n\tv_max3_f32 %0, %0, %5, %6\n\ts_nop 1\n\tv_max_f32_dpp %0, %0, %0 row_ror:8 row_mask:0xf bank_mask:0xf\n\t"
        "v_mov_b32 %1, %0\n\ts_nop 1\n\tv_permlane16_swap_b32 %0, %1\n\tv_max_f32 %0, %0, %1\n\t"
        "v_mov_b32 %1, %0\n\ts_nop 1\n\tv_permlane32_swap_b32 %0, %1\n\tv_max_f32 %0, %0, %1"
        : "=&v"(m), "=&v"(t) : "v"(a), "v"(b), "v"(c), "v"(d), "v"(e));
    return m;
}
#define SB() __builtin_amdgcn_sched_barrier(0)
DI float xmax32(float x) { auto r = __builtin_amdgcn_permlane32_swap(fu(x), fu(x), false, false); return fmx(uf(r[0]), uf(r[1])); }
DI float xmax16(float x) { auto r = __builtin_amdgcn_permlane16_swap(fu(x), fu(x), false, false); return fmx(uf(r[0]), uf(r[1])); }
DI float xadd32(float x) { auto r = __builtin_amdgcn_permlane32_swap(fu(x), fu(x), false, false); return uf(r[0]) + uf(r[1]); }
DI float xadd16(float x) { auto r = __builtin_amdgcn_permlane16_swap(fu(x), fu(x), false, false); return uf(r[0]) + uf(r[1]); }
DI u4 widen16(u2 x, u2 y) {
    auto a = __builtin_amdgcn_permlane16_swap(x[0], y[0], false, false);
    auto b = __builtin_amdgcn_permlane16_swap(x[1], y[1], false, false);
    u4 r = {a[0], b[0], a[1], b[1]}; return r;
}
DI u4 canon(u4 v) { u4 r; for (int i = 0; i < 4; ++i) r[i] = min(v[i], 0xFFFFFFFEu); return r; }
DI bool valid16(u4 v) { return max(max(v[0], v[1]), max(v[2], v[3])) != SENT; }
DI bool wall(bool p) { return __builtin_amdgcn_ballot_w64(p) == __builtin_amdgcn_ballot_w64(true); }
DI float ex2(float x) { return __builtin_amdgcn_exp2f(x); }

DI unsigned take_tag(unsigned ev) { unsigned t = __builtin_amdgcn_readfirstlane(ev) * 1664525u + 1013904223u; asm volatile("" : "+s"(t) :: "memory"); return t; }
DI void arrive(rsrc_t ws, unsigned tg, int tid) { if (tid == 0) st4<SC1>(tg, ws, W_ARR + blockIdx.x * 4); }
DI void setf(rsrc_t ws, unsigned tg, unsigned f, unsigned v) { st8<SC1>(u2{tg, v}, ws, W_FLAG + f * 8); }
DI unsigned getf(rsrc_t ws, unsigned tg, unsigned o) { u2 w = __builtin_amdgcn_raw_buffer_load_b64(ws, o, 0, SC1); return w[0] == tg ? w[1] : 0; }
DI void wait_prog(rsrc_t ws, unsigned tg, unsigned off, int n, unsigned need, unsigned& have, bool& dead, int lane) {
    if (have >= need || dead) return;
    unsigned o = W_FLAG + (off + (unsigned)(lane < n ? lane : 0)) * 8;
    for (unsigned sp = 0;; ++sp) {
        cfence();
        unsigned v = getf(ws, tg, o);
        if (wall(v >= need)) {
            unsigned h = need;
            if (wall(v >= need + 4)) h = need + 4; else if (wall(v >= need + 1)) h = need + 1;
            have = h; break;
        }
        if (sp > SPIN_MAX) { dead = true; break; }
        __builtin_amdgcn_s_sleep(4);
    }
    cfence();
}
DI u2 peek(rsrc_t ws, unsigned off, int n, int lane) { return __builtin_amdgcn_raw_buffer_load_b64(ws, W_FLAG + (off + (unsigned)(lane < n ? lane : 0)) * 8, 0, SC1); }
DI bool seen(u2 w, unsigned tg, unsigned need) { bool ok = wall(w[0] == tg && w[1] >= need); cfence(); return ok; }
DI void wait_slow(rsrc_t ws, unsigned tg, unsigned off, int n, unsigned need, int lane) {
    unsigned o = W_FLAG + (off + (unsigned)(lane < n ? lane : 0)) * 8;
#pragma clang loop unroll(disable)
    for (unsigned sp = 0; sp < SPIN_MAX; ++sp) {
        cfence();
        if (wall(getf(ws, tg, o) >= need)) break;
        __builtin_amdgcn_s_sleep(16);
    }
    cfence();
}
DI unsigned mx2(u4 v) { return max(v[0], v[2]); }
DI unsigned mx4(u4 v) { return max(max(v[0], v[1]), max(v[2], v[3])); }
DI bool anybad(unsigned m) { return __builtin_amdgcn_ballot_w64(m == SENT) != 0; }
DI unsigned rnow() { return (unsigned)__builtin_amdgcn_s_memrealtime(); }
DI void runtil(unsigned t) {
    asm volatile("1:\n\ts_memrealtime vcc\n\ts_waitcnt lgkmcnt(0)\n\ts_sub_u32 vcc_lo, vcc_lo, %0\n\ts_cmpk_lt_u32 vcc_lo, 0x400\n\ts_cbranch_scc1 1b"
                 :: "s"(__builtin_amdgcn_readfirstlane(t - 1024u)) : "vcc", "scc", "memory");
}
DI void nap(int n) { for (int i = 0; i < n; ++i) __builtin_amdgcn_s_sleep(1); }
DI unsigned am(unsigned o) { return ((o >> 13) << 14) | (o & 8191); }
DI rsrc_t mkring(void* ws) { return mk((void*)(((unsigned long long)ws + (2u << 20) - 1) & ~(unsigned long long)((2u << 20) - 1)), RING_BYTES); }
DI unsigned fhot(void* ws) { return (((0u - (unsigned)(size_t)ws) & ((2u << 20) - 1)) + PROBE + 4096 - W_FLAG) >> 3; }
DI unsigned wait_rdy(rsrc_t ws, unsigned tg, unsigned off, int n, int lane) {
    unsigned o = W_FLAG + (off + (unsigned)(lane < n ? lane : 0)) * 8, v;
#pragma clang loop unroll(disable)
    for (unsigned sp = 0; sp < SPIN_MAX; ++sp) {
        cfence();
        v = getf(ws, tg, o);
        if (wall(v != 0)) break;
        __builtin_amdgcn_s_sleep(2);
    }
    cfence();
    return (__builtin_amdgcn_readlane(v, 0) & 1) | ((__builtin_amdgcn_readlane(v, 1) & 1) << 1);
}

DI unsigned swzA(unsigned row) { return 2u * ((row & 1) | ((((row >> 1) ^ (row >> 2)) & 1) << 1) | (((row >> 3) & 1) << 2)); }
DI unsigned swzB(unsigned row) { return 2u * ((((row >> 1) ^ (row >> 2)) & 1) | (((row >> 3) & 1) << 1)); }
template <int KIND> DI void wload(u4 (&raw)[18], const void* w, int h, int tid) {
    if (KIND == 0) {
        rsrc_t r = mk(w, 576 * 4096 * 2);
        unsigned base = (unsigned)(tid >> 4) * 8192 + h * 256 + (tid & 15) * 16;
#pragma unroll
        for (int i = 0; i < 18; ++i) raw[i] = ld16<0>(r, base, i * 32 * 8192);
    } else {
        rsrc_t r = mk(w, 576 * 4096 * 2);
        unsigned base = (unsigned)h * 147456 + tid * 16;
#pragma unroll
        for (int i = 0; i < 18; ++i) raw[i] = ld16<0>(r, base, i * 8192);
    }
}
template <int KIND> DI void wstore(const u4 (&raw)[18], char* lds, int tid) {
    if (KIND == 0) {
        unsigned row = tid >> 4;
        char* p = lds + row * 256 + 16 * ((tid & 15) ^ swzA(row));
#pragma unroll
        for (int i = 0; i < 18; ++i) sts16(p + i * 32 * 256, raw[i]);
    } else {
#pragma unroll
        for (int i = 0; i < 18; ++i) {
            unsigned n = i * 512 + tid, row = n / 72, ch = n - row * 72;
            sts16(lds + row * 1152 + 16 * (ch ^ swzB(row)), raw[i]);
        }
    }
}
template <int KIND> DI const char* wpiece(const char* lds, unsigned row, unsigned ct, unsigned p) {
    unsigned ch = 2 * ct + (p >> 1);
    if (KIND == 0) return lds + row * 256 + 16 * (ch ^ swzA(row)) + 8 * (p & 1);
    return lds + row * 1152 + 16 * (ch ^ swzB(row)) + 8 * (p & 1);
}
template <int KIND> DI u8v wfrag_s(const char* lds, int j, int ct, int lane) {
    unsigned gb = lane >> 4, q = (lane >> 2) & 3, p = lane & 3;
    unsigned r0 = 16 * (gb >> 1) + 4 * (gb & 1) + (q & 1) + 8 * (q >> 1);
    u8v f;
#pragma unroll
    for (int u = 0; u < 4; ++u) {
        u2 v = ldtr(wpiece<KIND>(lds, r0 + 2 * (u & 1), ct, p) + (64 * j + 32 * (u >> 1)) * (KIND == 0 ? 256 : 1152));
        f[2 * u] = v[0]; f[2 * u + 1] = v[1];
    }
    return f;
}
template <int KIND> DI u4 wfrag_d(const char* lds, int j, int ct, int lane) {
    unsigned g = lane >> 4, q = (lane >> 2) & 3, p = lane & 3;
    u4 f;
#pragma unroll
    for (int u = 0; u < 2; ++u) {
        u2 v = ldtr(wpiece<KIND>(lds, 8 * g + 4 * u + q, ct, p) + (32 * j) * (KIND == 0 ? 256 : 1152));
        f[2 * u] = v[0]; f[2 * u + 1] = v[1];
    }
    return f;
}

#define UD 4
#define QD 4
#define UMID 4
#define UPRIO 1
constexpr int PUB0 = 12, PUB1 = 5, PUBX = 7, VSEG = 2;
constexpr int T_S = 208, T_PHI = 132, T_DZ = 4, T_NV = 8, T_NR = 10, T_E = 56, T_NA = -6, T_SW = 2, T_PA = 8;
constexpr int T_WD = 6;

struct Img {
    u4 v[5], v2[5];
    template <int N = 5> DI void load(const Args& a, unsigned r0, unsigned rs, int wv, int lane) {
        rsrc_t m0 = mk(a.mem0, 8 * 576 * 2), xs = mk(a.bx, 2048 * 576 * 2);
        unsigned o = (lane & 7) * 1152 + (lane >> 3) * 16, ox = r0 * 1152 + (lane >> 3) * 16;
        const unsigned e = wv == 7 ? 1024 : 0x40000000;
        v[0] = ld16<0>(m0, o + wv * 128); v2[0] = ld16<0>(m0, o + e);
#pragma unroll
        for (int j = 0; j < N - 1; ++j) { v[j + 1] = ld16<0>(xs, ox + wv * 128, j * rs * 1152); v2[j + 1] = ld16<0>(xs, ox + e, j * rs * 1152); }
    }
    template <int N = 5> DI void store(char* lds, int wv, int lane) {
#pragma unroll
        for (int j = 0; j < N; ++j) {
            char* p = lds + (j ? 40960 + j * 9216 : 0) + lane * 16;
            sts16(p + wv * 1024, v[j]);
            if (wv == 7) sts16(p + 8192, v2[j]);
        }
    }
};
DI void poll_M(rsrc_t rg, unsigned a1, unsigned a2, unsigned so, char* l1, char* l2, int wv, unsigned& budget) {
    u4 v = ld16<SC1>(rg, a1, so), v2 = {0, 0, 0, 0};
    if (wv == 7) v2 = ld16<SC1>(rg, a2, so);
    if (UNL(anybad(max(mx4(v), mx4(v2))))) {
#pragma clang loop unroll(disable)
        do { cfence(); v = ld16<SC1>(rg, a1, so); if (wv == 7) v2 = ld16<SC1>(rg, a2, so); } while (anybad(max(mx4(v), mx4(v2))) && budget != 0 && --budget != 0);
    }
    wait_vm();
    sts16(l1, v);
    if (wv == 7) sts16(l2, v2);
}
DI void poll_M4(rsrc_t rg, unsigned a1, unsigned a2, unsigned so, char* l1, char* l2, int wv, unsigned& budget) {
    u4 v = ld16<SC1>(rg, a1, so), v1 = ld16<SC1>(rg, a1 + 4096, so), v2 = {0, 0, 0, 0};
    if (wv == 3) v2 = ld16<SC1>(rg, a2, so);
    if (UNL(anybad(max(max(mx4(v), mx4(v1)), mx4(v2))))) {
#pragma clang loop unroll(disable)
        do { cfence(); v = ld16<SC1>(rg, a1, so); v1 = ld16<SC1>(rg, a1 + 4096, so); if (wv == 3) v2 = ld16<SC1>(rg, a2, so); }
        while (anybad(max(max(mx4(v), mx4(v1)), mx4(v2))) && budget != 0 && --budget != 0);
    }
    wait_vm();
    sts16(l1, v); sts16(l1 + 4096, v1);
    if (wv == 3) sts16(l2, v2);
}
DI unsigned sched_next(rsrc_t rg, unsigned at, unsigned so, unsigned g, unsigned s, int t_s) {
    u2 w = __builtin_amdgcn_raw_buffer_load_b64(rg, at, so, SC1);
    unsigned gw = __builtin_amdgcn_readfirstlane(w[0]), tag = __builtin_amdgcn_readfirstlane(w[1]);
    g = tag == s ? gw : g;
    return g + t_s + (g & 3) * 4;
}

DI void role_A(const Args& a, char* lds, int h, int rep, int tid, unsigned ev) {
    const int lane = tid & 63, wv = __builtin_amdgcn_readfirstlane(tid >> 6);
    rsrc_t ws = mk(a.ws, W_END), rg = mkring(a.ws);
    const u4 sv = {SENT, SENT, SENT, SENT};
    u8v wq[9], wk[9];
    unsigned tg;
    {
        unsigned o = PB + am((wv & 3) * 20480 + h * 640 + lane * 16);
        if (lane < 40) {
#pragma unroll
            for (int c = 0; c < 3; ++c) st16<SC1>(sv, rg, o + (wv >> 2) * 8192, c * PSL);
        }
        if (tid < 160) st16<SC1>(sv, ws, W_PP + (63 * 32 + h) * 2560 + tid * 16);
        u4 r1[18], r2[18];
        Img I;
        I.template load<1>(a, 0, 0, wv, lane);
        SB();
        wload<0>(r1, a.wq, h, tid); wload<0>(r2, a.wk, h, tid);
        tg = take_tag(ev);
        wstore<0>(r1, lds, tid); bar();
        arrive(ws, tg, tid);
#pragma unroll
        for (int j = 0; j < 9; ++j) wq[j] = wfrag_s<0>(lds, j, wv, lane);
        wait_vm();
        bar();
        if (tid == 0) { setf(ws, tg, F_RDYP + h, 1); }
        wstore<0>(r2, lds, tid);
        bar();
#pragma unroll
        for (int j = 0; j < 9; ++j) wk[j] = wfrag_s<0>(lds, j, wv, lane);
        bar();
        I.template store<1>(lds, wv, lane);
    }
    char* qk = lds + 10240;
    const unsigned q0 = (wait_rdy(ws, tg, F_RDYR, 36, lane) & 1) << 13;
    const int idx = (lane & 8) ? 0xEEEE : 0x4444;
    const int g4 = lane >> 4, col = lane & 15;
    unsigned budget = SPIN_MAX * 4;
    const unsigned a1 = MB + q0 + rep * 16384 + wv * 1024 + lane * 16;
    const unsigned a2 = MB + q0 + 65536 + rep * 1024 + lane * 16, at = MB + q0 + M_TS + rep * 128;
    char* const ll = lds + lane * 16;
    const char* const fp = qk + (4 * g4 + ((lane >> 2) & 3)) * 32 + 8 * (lane & 3);
    char* const qw = qk + (wv * 16 + col) * 32 + g4 * 8;
    const float cs = 0.08838834764831845f * 1.4426950408889634f;
    const int t_s = T_S;
    unsigned c0 = 0, c1 = 1, c2 = 2, tn = 0;
    auto proj = [&](unsigned img = 0) {
        bar();
        u4 m[9];
#pragma unroll
        for (int j = 0; j < 9; ++j) { m[j] = lds16(ll + img + j * 1024); SB(); }
        f4 dq = {0, 0, 0, 0}, dk = {0, 0, 0, 0};
        SB();
#pragma unroll
        for (int j = 0; j < 9; ++j) { dq = smf(m[j], wq[j], dq, idx); dk = smf(m[j], wk[j], dk, idx); SB(); }
        float t0 = swap32_add(dq[0], dk[0]), t1 = swap32_add(dq[1], dk[1]), t2 = swap32_add(dq[2], dk[2]), t3 = swap32_add(dq[3], dk[3]);
        sts8(qw, u2{pk(t0, t1), pk(t2, t3)});
    };
    auto next = [&](int s) {
        unsigned so = c1 * MSL;
        asm volatile("" : "+s"(so), "+s"(tn));
        if (s > 0) runtil(tn);
        poll_M4(rg, a1, a2, so, ll + wv * 1024, ll + 8192, wv, budget);
        unsigned c = c0; c0 = c1; c1 = c2; c2 = c;
    };
    if (wv == 0) {
        unsigned pa[4], pc[2];
#pragma unroll
        for (int r = 0; r < 4; ++r) pa[r] = PB + q0 + am(r * 20480 + h * 640 + g4 * 128 + (col & 7) * 16 + (col >> 3) * 8);
#pragma unroll
        for (int r = 0; r < 2; ++r) pc[r] = PB + q0 + am((2 * r + (g4 >> 1)) * 20480 + h * 640 + 512 + (col & 7) * 16 + (g4 & 1) * 8 + (col >> 3) * 4);
        __builtin_amdgcn_s_setprio(3);
#pragma clang loop unroll(disable)
        for (int s = 0; s < 63; ++s) {
            proj();
            u4 kx[8], F[4];
            const char* kb = ll + 16384 + (s & 1) * 16384;
#pragma unroll
            for (int i = 0; i < 8; ++i) kx[i] = lds16(kb + i * 1024);
            unsigned so = c0 * PSL;
            asm volatile("" : "+s"(so));
            bar();
#pragma unroll
            for (int kk = 0; kk < 4; ++kk) {
                u2 x = ldtr(fp + kk * 1024), y = ldtr(fp + kk * 1024 + 512);
                F[kk] = u4{x[0], x[1], y[0], y[1]};
                SB();
            }
            f4 t0 = {0, 0, 0, 0}, t1 = t0, tm = t0;
#pragma unroll
            for (int kk = 0; kk < 4; ++kk) { tm = mfma16(F[kk], F[kk], tm); t0 = mfma16(kx[kk], F[kk], t0); t1 = mfma16(kx[4 + kk], F[kk], t1); SB(); }
            float v[5];
            auto s01 = __builtin_amdgcn_permlane32_swap(fu(tm[0]), fu(tm[1]), false, false);
            auto s23 = __builtin_amdgcn_permlane32_swap(fu(tm[2]), fu(tm[3]), false, false);
            v[4] = uf(__builtin_amdgcn_update_dpp(s01[1], s23[1], 0x118, 0xf, 0xc, false));
#pragma unroll
            for (int i = 0; i < 4; ++i) v[i] = uf(__builtin_amdgcn_update_dpp(fu(t0[i]), fu(t1[i]), 0x118, 0xf, 0xc, false));
            float mc = -cs * redmax5(v[4], v[0], v[1], v[2], v[3]);
#pragma unroll
            for (int i = 0; i < 5; ++i) v[i] = ex2(__builtin_fmaf(v[i], cs, mc));
            float sum = (v[0] + v[1]) + (v[2] + v[3]) + v[4];
            sum += uf(__builtin_amdgcn_update_dpp(0u, fu(sum), 0x128, 0xf, 0xf, true));
            sum = xadd32(xadd16(sum));
            float r = __builtin_amdgcn_rcpf(sum);
            u2 px = {pk(v[0] * r, v[1] * r), pk(v[2] * r, v[3] * r)};
            st8<SC1>(px, rg, pa[0], so); st8<SC1>(px, rg, pa[1], so);
            float pmv = v[4] * r;
            auto sm = __builtin_amdgcn_permlane32_swap(fu(pmv), fu(pmv), false, false);
            unsigned pm = pk(uf(sm[0]), uf(sm[1]));
            st4<SC1>(pm, rg, pc[0], so); st4<SC1>(pm, rg, pc[1], so);
            st8<SC1>(px, rg, pa[2], so); st8<SC1>(px, rg, pa[3], so);
            tn = sched_next(rg, at, c0 * MSL, tn, tg + s, t_s);
            next(s);
        }
        __builtin_amdgcn_s_setprio(0);
        proj(); bar();
    } else if (wv < 3) {
#pragma clang loop unroll(disable)
        for (int s = 0;; ++s) {
            proj();
            bar();
            const char* kb = ll + 16384 + (s & 1) * 16384;
            f4 t0 = {0, 0, 0, 0}, t1 = t0, tm = t0;
#pragma unroll
            for (int kk = 0; kk < 4; ++kk) {
                u2 x = ldtr(fp + kk * 1024), y = ldtr(fp + kk * 1024 + 512);
                u4 qx = lds16(kb + 4096 + wv * 4096 + kk * 1024);
                t0 = mfma16(lds16(kb + kk * 1024), qx, t0); t1 = mfma16(lds16(kb + 4096 + kk * 1024), qx, t1); tm = mfma16(u4{x[0], x[1], y[0], y[1]}, qx, tm);
            }
            const float NEG = -1.0e9f * 1.4426950408889634f;
            float v[12];
#pragma unroll
            for (int i = 0; i < 4; ++i) {
                v[i] = t0[i] * cs; v[4 + i] = t1[i] * cs; v[8 + i] = g4 >= 2 ? tm[i] * cs : -3.0e38f;
                bool tri = (4 * g4 + i) <= col;
                if (wv == 1) { v[i] = tri ? v[i] : NEG; v[4 + i] = NEG; }
                else { v[4 + i] = tri ? v[4 + i] : NEG; }
            }
            float m = v[0];
#pragma unroll
            for (int i = 1; i < 12; ++i) m = fmx(m, v[i]);
            m = xmax32(xmax16(m));
            float sum = 0;
#pragma unroll
            for (int i = 0; i < 12; ++i) { v[i] = ex2(v[i] - m); sum += v[i]; }
            sum = xadd32(xadd16(sum));
            float r = __builtin_amdgcn_rcpf(sum);
            u4 px = {pk(v[0] * r, v[1] * r), pk(v[2] * r, v[3] * r), pk(v[4] * r, v[5] * r), pk(v[6] * r, v[7] * r)};
            u2 pm2 = {pk(v[8] * r, v[9] * r), pk(v[10] * r, v[11] * r)};
            u4 pm = widen16(pm2, pm2);
            unsigned b = W_PP + (unsigned)(s * 32 + h) * 2560;
            st16<SC1>(px, ws, (wv - 1) * 1024 + lane * 16, b);
            if (g4 == 2) st16<SC1>(pm, ws, 2048 + (wv - 1) * 256 + col * 16, b);
            if (s == 63) break;
            tn = sched_next(rg, at, c0 * MSL, tn, tg + s, t_s);
            next(s);
        }
    } else if (wv == 3) {
        const unsigned pr = lane < 40 ? h * 640 + lane * 16 : 0x40000000u;
#pragma clang loop unroll(disable)
        for (int s = 0;; ++s) {
            proj();
            bar();
            if (s == 63) break;
            tn = sched_next(rg, at, c0 * MSL, tn, tg + s, t_s);
            nap(8);
#pragma unroll
            for (int r = 0; r < 4; ++r) st16<SC1>(sv, rg, PB + q0 + am(pr + r * 20480), c2 * PSL);
            next(s);
        }
    } else {
        bool dead = false, ok = false, pend = false; unsigned khave = 0;
        const int t_na = T_NA;
        const unsigned fk = fhot(a.ws) + (q0 >> 3) + F_KPROG + h;
        const unsigned kg = (wv < 6 ? W_KX : W_QX) + h * 8192 + (wv & 1) * 4096 + lane * 16;
        u4 t[4]; u2 fl;
        auto issue = [&](int s, unsigned off) {
#pragma unroll
            for (int i = 0; i < 4; ++i) t[i] = ld16<SC1>(ws, off + i * 1024, (unsigned)s * 32 * 8192);
            fl = peek(ws, fk, 1, lane);
            SB();
        };
        auto put = [&](int s) {
            char* kb = ll + 16384 + (s & 1) * 16384 + (wv - 4) * 4096;
#pragma unroll
            for (int i = 0; i < 4; ++i) sts16(kb + i * 1024, t[i]);
            ok = seen(fl, tg, s + 2);
        };
        wait_prog(ws, tg, fk, 1, 1, khave, dead, lane);
        issue(0, kg);
        put(0);
#pragma clang loop unroll(disable)
        for (int s = 0;; ++s) {
            proj();
            if (s == 63 && tid == 256) setf(ws, tg, F_PPROG + h, 63);
            bar();
            if (s == 63) break;
            if (!pend) {
                nap(t_na);
                if (UNL(!ok)) wait_prog(ws, tg, fk, 1, s + 2, khave, dead, lane);
                issue(s + 1, kg);
            }
            if (t_na < 0) tn = sched_next(rg, at, c0 * MSL, tn, tg + s, t_s);
            put(s + 1);
            pend = t_na < 0 && ok && (int)(tn - rnow()) > 0 && (int)(tn - rnow()) < 300;
            if (pend) { runtil(tn - t_na); issue(s + 2, kg); }
            unsigned c = c0; c0 = c1; c1 = c2; c2 = c;
        }
    }
}

DI void role_V(const Args& a, char* lds, int h, int rep, int tid, unsigned ev) {
    const int lane = tid & 63, wv = __builtin_amdgcn_readfirstlane(tid >> 6);
    rsrc_t ws = mk(a.ws, W_END), rg = mkring(a.ws);
    const u4 sv = {SENT, SENT, SENT, SENT};
    const int g4 = lane >> 4, col = lane & 15;
    const unsigned t4 = wv + 8 * (((g4 & 1) << 1) | (g4 >> 1)), t5 = 32 + (wv & 3);
    unsigned o4 = ZB + t4 * 16384 + h * 256 + col * 16, o5 = ZB + t5 * 16384 + h * 256 + col * 16;
    const unsigned z4 = W_ZA + (t4 * 32 + h) * 256 + col * 16, z5 = W_ZA + (t5 * 32 + h) * 256 + col * 16;
    u8v wvv[9], wo[5][2];
    unsigned tg;
    {
#pragma unroll
        for (int c = 0; c < 6; ++c) { st16<SC1>(sv, rg, o4, (c >> 1) * ZSL + (c & 1) * 8192); if (g4 == 0) st16<SC1>(sv, rg, o5, (c >> 1) * ZSL + (c & 1) * 8192); }
        u4 r1[18], r2[18];
        Img I;
        I.load(a, 32 * VSEG + (lane & 3) + (lane & 4) * 4, 4, wv, lane);
        SB();
        wload<0>(r1, a.wv, h, tid); wload<1>(r2, a.wo, h, tid);
        tg = take_tag(ev);
        wstore<0>(r1, lds, tid); bar();
        arrive(ws, tg, tid);
#pragma unroll
        for (int j = 0; j < 9; ++j) wvv[j] = wfrag_s<0>(lds, j, wv, lane);
        wait_vm();
        bar();
        if (tid == 0) { setf(ws, tg, F_RDYP + 32 + h, 1); }
        wstore<1>(r2, lds, tid);
        bar();
#pragma unroll
        for (int i = 0; i < 5; ++i)
#pragma unroll
            for (int kk = 0; kk < 2; ++kk) wo[i][kk] = wfrag_s<1>(lds, kk, i < 4 ? wv + 8 * i : 32 + (wv & 3), lane);
        bar();
        I.store(lds, wv, lane);
    }
    char* mimg = lds; char* vimg = lds + 10240;
    const unsigned par = wait_rdy(ws, tg, F_RDYR, 36, lane);
    const unsigned q0 = (par & 1) << 13;
    o4 += (par & 2) << 12; o5 += (par & 2) << 12;
    const int idx = (lane & 8) ? 0xEEEE : 0x4444;
    const unsigned a1 = MB + q0 + rep * 16384 + wv * 1024 + lane * 16;
    const unsigned a2 = MB + q0 + 65536 + rep * 1024 + lane * 16, at = MB + q0 + M_TS + rep * 128;
    char* const l1 = mimg + wv * 1024 + lane * 16;
    char* const l2 = mimg + 8192 + lane * 16;
    char* const vw = vimg + (wv * 16 + col) * 16 + (g4 & 1) * 8 + (g4 >> 1) * 4;
    const char* const vr = vimg + (16 * g4 + 8 * ((lane >> 1) & 1) + ((lane >> 2) & 3)) * 16 + 8 * (lane & 1);
    const int t_s = T_S, t_nv = T_NV;
    unsigned budget = SPIN_MAX * 4;
    unsigned c0 = 0, c1 = 1, c2 = 2, tn = 0;
    u4 o, o5v;
    auto ph1 = [&](unsigned img, unsigned vo) {
        f4 d = {0, 0, 0, 0}, d2 = d;
        u4 m[9];
#pragma unroll
        for (int j = 0; j < 9; ++j) { m[j] = lds16(mimg + img + j * 1024 + lane * 16); SB(); }
        SB();
#pragma unroll
        for (int j = 0; j < 9; ++j) { if (j & 1) d2 = smf(m[j], wvv[j], d2, idx); else d = smf(m[j], wvv[j], d, idx); SB(); }
        d += d2;
        sts4(vw + vo, pk(swap32_add(d[0], d[2]), swap32_add(d[1], d[3])));
    };
    auto ph2 = [&](unsigned vo, rsrc_t r, unsigned a4, unsigned a5, unsigned so) {
        u4 av[2];
#pragma unroll
        for (int kk = 0; kk < 2; ++kk) {
            u2 x = ldtr(vr + vo + kk * 1024), y = ldtr(vr + vo + kk * 1024 + 64);
            av[kk] = u4{x[0], x[1], y[0], y[1]};
        }
        f4 z[5];
#pragma unroll
        for (int i = 0; i < 4; ++i) z[i] = smf(av[0], wo[i][0], f4{0, 0, 0, 0}, idx);
#pragma unroll
        for (int i = 0; i < 4; ++i) z[i] = smf(av[1], wo[i][1], z[i], idx);
        z[4] = f4{0, 0, 0, 0};
        if (wv < 4) { z[4] = smf(av[0], wo[4][0], z[4], idx); z[4] = smf(av[1], wo[4][1], z[4], idx); }
        u2 X = {pk(swap32_add(z[0][0], z[1][0]), swap32_add(z[0][1], z[1][1])), pk(swap32_add(z[0][2], z[1][2]), swap32_add(z[0][3], z[1][3]))};
        u2 Y = {pk(swap32_add(z[2][0], z[3][0]), swap32_add(z[2][1], z[3][1])), pk(swap32_add(z[2][2], z[3][2]), swap32_add(z[2][3], z[3][3]))};
        o = widen16(X, Y);
        st16<SC1>(o, r, a4, so);
        o5v = o;
        if (wv < 4) {
            u2 Z = {pk(xadd32(z[4][0]), xadd32(z[4][1])), pk(xadd32(z[4][2]), xadd32(z[4][3]))};
            o5v = widen16(Z, Z);
            if (g4 == 0) st16<SC1>(o5v, r, a5, so);
        }
    };
#pragma clang loop unroll(disable)
    for (int s = 0;; ++s) {
        bar();
        ph1(0, 0);
        if (s == 63 && tid == 0) setf(ws, tg, F_ZPROG + h, 63);
        bar();
        ph2(0, rg, o4, o5, c0 * ZSL);
        if (s == 63) break;
        tn = sched_next(rg, at, c0 * MSL, tn, tg + s, t_s);
        if (s) nap(t_nv);
        st16<SC1>(o, ws, z4, (unsigned)s * Z_SLOT);
        st16<SC1>(sv, rg, o4, c2 * ZSL);
        if (wv < 4 && g4 == 0) { st16<SC1>(o5v, ws, z5, (unsigned)s * Z_SLOT); st16<SC1>(sv, rg, o5, c2 * ZSL); }
        if (UNL(s == 0)) {
#pragma clang loop unroll(disable)
            for (int j = 1; j < 5; ++j) ph1(40960 + j * 9216, j * 2048);
            bar();
#pragma unroll 2
            for (int j = 1; j < 5; ++j) ph2(j * 2048, ws, W_U + (t4 * 32 + h) * 1024 + col * 16, W_U + (t5 * 32 + h) * 1024 + col * 16, VSEG * 36 * 32768 + j * 256 - 256);
            wait_vm();
            bar();
            if (tid == 0) { setf(ws, tg, F_U0 + h, 1); }
        }
        unsigned so = c1 * MSL;
        asm volatile("" : "+s"(so), "+s"(tn));
        if (s > 0) runtil(tn);
        poll_M(rg, a1, a2, so, l1, l2, wv, budget);
        unsigned c = c0; c0 = c1; c1 = c2; c2 = c;
    }
}

DI void role_R(const Args& a, char* lds, int T, int rep, int tid, unsigned ev) {
    const int lane = tid & 63, wv = __builtin_amdgcn_readfirstlane(tid >> 6);
    rsrc_t ws = mk(a.ws, W_END), rg = mkring(a.ws);
    const int g4 = lane >> 4, half = (lane >> 3) & 1, r = lane & 7;
    unsigned mo = MB + (T < 32 ? (wv & 3) * 16384 + T * 256 : 65536 + (wv & 3) * 1024 + (T - 32) * 256) + lane * 4;
#pragma unroll
    for (int c = 0; c < 3; ++c) st4<SC1>(SENT, rg, mo + (wv >> 2) * 8192, c * MSL);
    unsigned rdy = 2;
    const unsigned tg = take_tag(ev);
    wait_vm();
    bar();
    arrive(ws, tg, tid);
    if (T < 2 && wv == 0) {
        unsigned o = T * ZB + PROBE;
        if (lane < 16) st4<SC1>(0, rg, o + (lane & 1) * 8192 + (lane >> 1) * 256);
        wait_vm();
        unsigned b0 = ~0u, b1 = ~0u;
#pragma clang loop unroll(disable)
        for (int i = 0; i < 10; ++i) {
            unsigned t0 = (unsigned)__builtin_amdgcn_s_memtime();
            asm volatile("s_waitcnt lgkmcnt(0)" ::: "memory");
            unsigned v = ld4<SC1>(rg, o + (i & 1) * 8192 + (i >> 1) * 256);
            asm volatile("s_waitcnt vmcnt(0)" :: "v"(v) : "memory");
            unsigned dt = (unsigned)__builtin_amdgcn_s_memtime() - t0;
            if (i & 1) b1 = min(b1, dt); else b0 = min(b0, dt);
        }
        rdy |= b1 < b0 ? 1 : 0;
    }
    if (tid == 0) setf(ws, tg, F_RDYR + T, rdy);
    if (T < 2) bar();
    const unsigned par = wait_rdy(ws, tg, F_RDYR, 2, lane);
    const unsigned q0 = (par & 1) << 13;
    mo += q0;
    unsigned budget = SPIN_MAX * 4;
    char* const ul = lds + 16384 + (wv & 3) * 8192 + lane * 16;
    const int t_s = T_S, t_phi = T_PHI, t_dz = T_DZ, t_nr = T_NR, t_e = T_E, t_sw = T_SW;
    unsigned c0 = 0, c1 = 1, c2 = 2, G = 0;
    u4 uf_[8];
    if (wv < 4) {
        wait_rdy(ws, tg, F_RDYP, 64, lane);
        unsigned ap[4];
#pragma unroll
        for (int i = 0; i < 4; ++i) ap[i] = PB + q0 + am(rep * 20480 + (8 * wv + 2 * i + half) * 640 + g4 * 128 + r * 16);
        const unsigned am_ = PB + q0 + am(rep * 20480 + (8 * wv + 4 * half + g4) * 640 + 512 + r * 16);
        const unsigned az = ZB + ((par & 2) << 12) + T * 16384 + wv * 2048 + lane * 16;
        char* const rw = lds + ((g4 >> 1) * 32 + r * 4 + (g4 & 1) * 2) * 48 + wv * 8;
        const char* const rr = lds + lane * 48;
        const unsigned at = MB + q0 + M_TS + rep * 128;
        bar();
#pragma clang loop unroll(disable)
        for (int s = 0; s < 63; ++s) {
            if (s == 1 || s == 2) bar();
#pragma unroll
            for (int i = 0; i < 8; ++i) uf_[i] = lds16(ul + (s & 1) * 32768 + i * 1024);
            unsigned sz = c0 * ZSL, sp = c0 * PSL, tp = G + t_phi + (G & 2) * 2 + (G & G >> 1 & 1) * 4, tz = tp - t_dz, sm = c1 * MSL;
            LDSP u2* w_ = (LDSP u2*)(rw + (s & 1) * 4096); lp r_ = (lp)(rr + (s & 1) * 4096);
            asm volatile("" : "+v"(w_), "+v"(r_));
            asm volatile("s_waitcnt lgkmcnt(0)" : "+s"(sz), "+s"(sp), "+s"(tz), "+s"(tp), "+s"(sm), "+v"(uf_[0]), "+v"(uf_[1]), "+v"(uf_[2]), "+v"(uf_[3]), "+v"(uf_[4]), "+v"(uf_[5]), "+v"(uf_[6]), "+v"(uf_[7]));
            if (s > 0) runtil(tz);
            u4 z0 = ld16<SC1>(rg, az, sz), z1 = ld16<SC1>(rg, az + 1024, sz);
            cfence();
            if (s > 0) runtil(tp);
            u4 p[4], pm = ld16<SC1>(rg, am_, sp);
#pragma unroll
            for (int i = 0; i < 4; ++i) p[i] = ld16<SC1>(rg, ap[i], sp);
            const f4 zero = {0, 0, 0, 0};
            f4 b0, b1;
            auto mm = [&]() {
                SB();
                b0 = mfma16(z0, pm, zero); b1 = mfma16(z1, pm, zero);
#pragma unroll
                for (int i = 0; i < 4; ++i) { SB(); b0 = mfma16(uf_[2 * i], p[i], b0); b1 = mfma16(uf_[2 * i + 1], p[i], b1); }
                SB();
            };
            auto bad = [&]() { return anybad(max(max(max(mx2(z0), mx2(z1)), mx4(pm)), max(max(mx2(p[0]), mx2(p[1])), max(mx2(p[2]), mx2(p[3]))))); };
            mm();
            if (UNL(bad())) {
#pragma clang loop unroll(disable)
                do {
                    cfence();
                    z0 = ld16<SC1>(rg, az, sz); z1 = ld16<SC1>(rg, az + 1024, sz); pm = ld16<SC1>(rg, am_, sp);
#pragma unroll
                    for (int i = 0; i < 4; ++i) p[i] = ld16<SC1>(rg, ap[i], sp);
                } while (bad() && budget != 0 && --budget != 0);
                mm();
            }
            f4 sel;
#pragma unroll
            for (int i = 0; i < 4; ++i) sel[i] = b0[i] + uf(__builtin_amdgcn_update_dpp(0u, fu(b1[i]), 0x128, 0xf, 0xf, true));
            if (half == 0) { w_[0] = u2{fu(sel[0]), fu(sel[1])}; w_[6] = u2{fu(sel[2]), fu(sel[3])}; }
            bar();
            f4 t = __builtin_bit_cast(f4, lds16(r_)) + __builtin_bit_cast(f4, lds16(r_ + 16));
            st4<SC1>(pk(t[0] + t[2], t[1] + t[3]), rg, mo, sm);
            st4<SC1>(SENT, rg, mo, c0 * MSL);
            G += t_s + (G & 3) * 4;
#pragma clang loop unroll(disable)
            for (int i = 0; i < 12; ++i) {
                cfence();
                const unsigned tt = rnow();
                u2 w = __builtin_amdgcn_raw_buffer_load_b64(rg, at, sm, SC1);
                if (__builtin_amdgcn_readfirstlane(w[1]) == tg + s + 1) { G = __builtin_amdgcn_readfirstlane(w[0]); break; }
                if (s > 0 && (unsigned)(G + 8 - tt) < 64u) break;
            }
            wait_vm();
            unsigned c = c0; c0 = c1; c1 = c2; c2 = c;
        }
    } else {
        bool dead = false, ok = false, okv = false; unsigned uhave = 0;
        const unsigned ub = W_U + (unsigned)(T * 32 + 8 * (wv & 3)) * 1024 + lane * 16, fo = fhot(a.ws) + (q0 >> 3) + F_UPROG + (wv & 3) * 8;
        auto fetch = [&](int s) {
            if (UNL(!ok)) wait_prog(ws, tg, fo, 8, s + 1, uhave, dead, lane);
#pragma unroll
            for (int i = 0; i < 8; ++i) uf_[i] = ld16<SC1>(ws, ub + (i & 3) * 1024, (unsigned)s * 36 * 32768 + (i >> 2) * 4096);
            const u2 fl = peek(ws, fo, 8, lane);
#pragma unroll
            for (int i = 0; i < 8; ++i) sts16(ul + (s & 1) * 32768 + i * 1024, uf_[i]);
            ok = seen(fl, tg, s + 2);
        };
        unsigned nf = 0; bool was = false;
        fetch(0);
        bar();
#pragma clang loop unroll(disable)
        for (int s = 0; s < 63; ++s) {
            unsigned sm = c1 * MSL;
            asm volatile("" : "+s"(sm));
            bar();
            if (T == 0 && wv == 4) {
                const unsigned t = rnow();
                G += t_s + (G & 3) * 4;
                const bool late = s == 0 || (int)(t - G) > 0;
                if (late) { G = t + t_e; nf += s >= 8 && !was; }
                was = late;
                G = (G & ~3u) | min(nf * t_sw >> 2, 3u);
                if (lane < 4) st8<SC1>(u2{G, tg + s + 1}, rg, MB + q0 + M_TS + lane * 128, sm);
            }
            if (T == 0 && tid == 320) setf(ws, tg, F_CPROG, s);
            unsigned c = c0; c0 = c1; c1 = c2; c2 = c;
            if (s == 0) {
                const unsigned t = rnow();
                fetch(1);
                bar();
#pragma clang loop unroll(disable)
                for (int i = 0; i < 8; ++i) {
                    cfence();
                    const u2 f1 = peek(ws, F_U0 + (wv & 3) * 8, 8, lane), f2 = peek(ws, fo, 8, lane);
                    okv = seen(f1, tg, 1); ok = seen(f2, tg, 4);
                    if ((okv && ok) || (int)(rnow() - t) > 180) break;
                }
            } else if (s == 1) {
                u4 ug[8];
                if (UNL(!okv)) wait_rdy(ws, tg, F_U0 + (wv & 3) * 8, 8, lane);
#pragma unroll
                for (int i = 0; i < 8; ++i) ug[i] = ld16<SC1>(ws, ub + (i & 3) * 1024, 2 * 36 * 32768 + (i >> 2) * 4096);
                fetch(3);
#pragma unroll
                for (int i = 0; i < 8; ++i) sts16(ul + i * 1024, ug[i]);
                bar();
            } else {
                nap(t_nr);
                if (s < 61) fetch(s + 2);
            }
        }
    }
}

struct XS {
    rsrc_t xs; unsigned vo, so[5], lo[5]; int wv;
    lp e0, e1, base;
    DI XS(const void* x, char* lds, int wv_, int lane) : wv(wv_) {
        xs = mk(x, 2048 * 576 * 2);
        vo = (lane >> 3) * 1152 + (((lane & 7) ^ (lane >> 3)) * 16);
#pragma unroll
        for (int i = 0; i < 5; ++i) { unsigned f = i < 4 ? wv + 8 * i : 32 + (wv & 3), rb = f / 9; so[i] = rb * 9216 + (f - 9 * rb) * 128; lo[i] = (unsigned)(size_t)(lp)lds + f * 1024; }
        unsigned o = ((lane >> 3) & 1) * 9216 + (lane & 7) * 128 + (((lane >> 4) ^ (lane & 7)) * 16);
        e0 = (lp)lds + o; e1 = (lp)lds + (o ^ 64); base = (lp)lds;
    }
    DI void load(u4 (&r)[5], int seg = 1) { for (int i = 0; i < 5; ++i) r[i] = ld16<0>(xs, vo, so[i] + seg * 36864); }
    DI void store(const u4 (&r)[5], int lane) { for (int i = 0; i < 5; ++i) *(LDSP u4*)(size_t)(lo[i] + lane * 16) = r[i]; }
    DI void dma1(int i, int seg, unsigned buf) {
        asm volatile("s_mov_b32 m0, %0\n\ts_nop 0\n\tbuffer_load_dwordx4 %1, %2, %3 offen lds" :: "s"(lo[i] + buf), "v"(vo), "s"(xs), "s"(so[i] + seg * 36864) : "memory");
        SB();
    }
    DI void dma(int seg, unsigned buf) {
#pragma unroll
        for (int i = 0; i < 5; ++i)
            if (i < 4 || wv < 4) dma1(i, seg, buf);
    }
    DI void dma9(int seg, unsigned buf) {
        const unsigned l = (unsigned)(size_t)base + buf + (wv & 3) * 9216, g = seg * 36864 + (wv & 3) * 9216;
#pragma unroll
        for (int i = 0; i < 9; ++i)
            asm volatile("s_mov_b32 m0, %0\n\ts_nop 0\n\tbuffer_load_dwordx4 %1, %2, %3 offen lds" :: "s"(l + i * 1024), "v"(vo), "s"(xs), "s"(g + i * 128) : "memory");
        SB();
    }
    DI u4 frag(unsigned buf, int rt, int j) const { return lds16(((j & 1) ? e1 : e0) + buf + rt * 18432 + (j >> 1) * 1024); }
};

DI unsigned role_QK(const Args& a, char* lds, int h, int tid, unsigned ev) {
    const int lane = tid & 63, wv = __builtin_amdgcn_readfirstlane(tid >> 6);
    rsrc_t ws = mk(a.ws, W_END);
    XS X(a.bx, lds, wv, lane);
    u4 w[2][18];
    unsigned tg;
    {
        u4 r1[18], r2[18], xr[5];
        rsrc_t rq = mk(a.wq, 576 * 4096 * 2), rk = mk(a.wk, 576 * 4096 * 2);
        const unsigned base = (unsigned)(tid >> 4) * 8192 + h * 256 + (tid & 15) * 16;
#pragma unroll
        for (int i = 0; i < 18; ++i) r1[i] = ld16<0>(i < 9 ? rq : rk, base, (i % 9) * 32 * 8192);
#pragma unroll
        for (int i = 0; i < 18; ++i) r2[i] = ld16<0>(i < 9 ? rq : rk, base, (9 + i % 9) * 32 * 8192);
        tg = take_tag(ev);
        wstore<0>(r1, lds, tid); bar();
        arrive(ws, tg, tid);
        const char* wl = lds + (wv >> 2) * 9 * 8192;
#pragma unroll
        for (int j = 0; j < 9; ++j) { w[0][j] = wfrag_d<0>(wl, j, 2 * (wv & 3), lane); w[1][j] = wfrag_d<0>(wl, j, 2 * (wv & 3) + 1, lane); }
        X.load(xr, 0);
        bar();
        wstore<0>(r2, lds, tid); bar();
#pragma unroll
        for (int j = 0; j < 9; ++j) { w[0][9 + j] = wfrag_d<0>(wl, j, 2 * (wv & 3), lane); w[1][9 + j] = wfrag_d<0>(wl, j, 2 * (wv & 3) + 1, lane); }
        bar();
        X.store(xr, lane);
    }
    X.dma(1, 36864);
    bar();
    const unsigned ob = (wv < 4 ? W_QX : W_KX) + (unsigned)h * 8192 + (wv & 3) * 1024 + lane * 16;
    unsigned b0 = 0, b1 = 36864, b2 = 73728;
    u4 o[2];
    const unsigned fk = fhot(a.ws) + (tid & 1) * 1024 + F_KPROG + h;
    const int pubx = PUBX, pub0 = PUB0;
#pragma clang loop unroll(disable)
    for (int s = 0; s < 64; ++s) {
        const bool early = wv >= 4 && s >= pub0;
        auto xfer = [&](int so) {
            wait_vm();
            X.dma(min(s + 2, 63), b2);
            st16<SC1>(o[0], ws, ob, (unsigned)so * 32 * 8192);
            st16<SC1>(o[1], ws, ob + 4096, (unsigned)so * 32 * 8192);
            SB();
        };
        if (early) xfer(s - 1);
        f4 c[2][2] = {};
        u4 x0[18], x1[18];
#pragma unroll
        for (int j = -QD; j < 18; ++j) {
#pragma unroll
            for (int i = 0; i < 4; ++i) {
                if (j >= 0) c[i >> 1][i & 1] = mfma16(w[i & 1][j], i < 2 ? x0[j] : x1[j], c[i >> 1][i & 1]);
                SB();
                if (j + QD < 18) { if (i == 0) x0[j + QD] = X.frag(b0, 0, j + QD); if (i == 2) x1[j + QD] = X.frag(b0, 1, j + QD); }
                SB();
            }
        }
#pragma unroll
        for (int rt = 0; rt < 2; ++rt) o[rt] = u4{pk(c[rt][0][0], c[rt][0][1]), pk(c[rt][0][2], c[rt][0][3]), pk(c[rt][1][0], c[rt][1][1]), pk(c[rt][1][2], c[rt][1][3])};
        asm volatile("" : "+v"(o[0]), "+v"(o[1]));
        if (!early) xfer(s);
        if (s < pubx) wait_vm();
        bar();
        if (tid < 2) { unsigned v = s < pubx ? s + 1 : s < pub0 ? s : s - 1; setf(ws, tg, fk, v); }
        unsigned t = b0; b0 = b1; b1 = b2; b2 = t;
    }
    st16<SC1>(o[0], ws, ob, 63u * 32 * 8192);
    st16<SC1>(o[1], ws, ob + 4096, 63u * 32 * 8192);
    wait_vm();
    bar();
    if (tid < 2) setf(ws, tg, fk, 64);
    return tg;
}

DI void role_U(const Args& a, char* lds, int h, int tid, unsigned ev) {
    const int lane = tid & 63, wv = __builtin_amdgcn_readfirstlane(tid >> 6);
    rsrc_t ws = mk(a.ws, W_END);
    XS X(a.bx, lds, wv, lane);
    u4 w[2][9], wo[5][4];
    unsigned tg;
    {
        u4 r1[18], r2[18], xr[5];
        X.load(xr, 0);
        SB();
        wload<0>(r1, a.wv, h, tid); wload<1>(r2, a.wo, h, tid);
        tg = take_tag(ev);
        wstore<0>(r1, lds, tid); bar();
        arrive(ws, tg, tid);
#pragma unroll
        for (int j = 0; j < 18; ++j) w[j & 1][j >> 1] = wfrag_d<0>(lds + (wv >> 2) * 9 * 8192, j >> 1, 2 * (wv & 3) + (j & 1), lane);
        bar();
        wstore<1>(r2, lds, tid); bar();
#pragma unroll
        for (int i = 0; i < 5; ++i)
#pragma unroll
            for (int kk = 0; kk < 4; ++kk) wo[i][kk] = wfrag_d<1>(lds, kk, i < 4 ? wv + 8 * i : 32 + (wv & 3), lane);
        bar();
        X.store(xr, lane);
    }
    X.dma(1, 36864);
    bar();
    const int g4 = lane >> 4, col = lane & 15;
    char* const vw = lds + 110592 + (wv & 3) * 1024 + ((g4 >> 1) * 16 + col) * 16 + (g4 & 1) * 8;
    char* const pw = lds + 126976 + (wv & 3) * 4096 + lane * 16;
    const char* const vr = lds + 110592 + lane * 16;
    const unsigned uo = W_U + (unsigned)wv * 32768 + (unsigned)h * 1024 + lane * 16;
    unsigned b0 = 0, b1 = 36864, b2 = 73728;
    const int pub1 = PUB1;
    const unsigned fu_ = fhot(a.ws) + (tid & 1) * 1024 + F_UPROG + h;
    auto sg = [](int i) { return i + (i >= VSEG); };
    u4 x0[9], x1[9], va[2][4];
    f4 c[4];
    auto xpre = [&](unsigned b, int kh) {
#pragma unroll
        for (int j = 0; j < UD; ++j) { x0[j] = X.frag(b, 0, 9 * kh + j); x1[j] = X.frag(b, 1, 9 * kh + j); }
        SB();
    };
    auto vload = [&](int s, int i) { va[0][i] = lds16(vr + ((s + 1) & 1) * 8192 + i * 1024); va[1][i] = lds16(vr + ((s + 1) & 1) * 8192 + 4096 + i * 1024); };
    auto vproj = [&](int s, int kh) {
#pragma unroll
        for (int j = 0; j < 9; ++j) {
            if (j == 5 && kh == 1) bar();
#pragma unroll
            for (int i = 0; i < 4; ++i) {
                c[i] = mfma16(w[i & 1][j], i < 2 ? x0[j] : x1[j], j ? c[i] : f4{0, 0, 0, 0});
                SB();
                if (j + UD < 9) { if (i == 0) x0[j + UD] = X.frag(b0, 0, 9 * kh + j + UD); if (i == 2) x1[j + UD] = X.frag(b0, 1, 9 * kh + j + UD); }
                if (j >= 5 && kh == 0 && !(i & 1)) va[i >> 1][j - 5] = lds16(vr + ((s + 1) & 1) * 8192 + (i >> 1) * 4096 + (j - 5) * 1024);
                if (j == 5 && kh == 1) va[0][i] = lds16(pw + i * 1024);
                SB();
            }
        }
    };
    auto uproj = [&](int s, int n) {
        const unsigned so = (unsigned)sg(s - 1) * 36 * 32768;
        f4 d[5][2];
#pragma unroll
        for (int i = 0; i <= n; ++i) {
            if (i == UMID && n == 5) {
                bar();
                if (tid < 2 && s >= pub1) { setf(ws, tg, fu_, sg(s - 2) + 1); }
            }
            if (i == n - 1) xpre(n == 5 ? b1 : b0, n == 4);
            if (i < n) {
#pragma unroll
                for (int kk = 0; kk < 4; ++kk)
#pragma unroll
                    for (int r = 0; r < 2; ++r) d[i][r] = mfma16(va[r][kk], wo[i][kk], kk ? d[i][r] : f4{0, 0, 0, 0});
                SB();
            }
            if (i == 0 && n == 5) wait_vm();
            if (i > 0) {
                const f4 p = d[i - 1][0], q = d[i - 1][1];
                st16<SC1>(u4{pk(p[0], p[1]), pk(p[2], p[3]), pk(q[0], q[1]), pk(q[2], q[3])}, ws, uo + so, (i - 1) * 8 * 32768);
                SB();
            }
        }
    };
    if (wv < 4) {
        xpre(b0, 0);
        __builtin_amdgcn_s_setprio(UPRIO);
        vproj(0, 0);
#pragma unroll
        for (int i = 0; i < 4; ++i) sts16(pw + i * 1024, __builtin_bit_cast(u4, c[i]));
        wait_vm();
        bar();
        bar();
        b0 = 36864; b1 = 73728; b2 = 0;
        xpre(b0, 0);
#pragma clang loop unroll(disable)
        for (int s = 1; s < 64; ++s) {
            vproj(s, 0);
#pragma unroll
            for (int i = 0; i < 4; ++i) sts16(pw + i * 1024, __builtin_bit_cast(u4, c[i]));
            SB();
            uproj(s, 5);
            if (s < pub1 || s == 63) wait_vm();
            bar();
            if (tid < 2 && (s < pub1 || s == 63)) { setf(ws, tg, fu_, sg(s - 1) + 1); }
            unsigned t = b0; b0 = b1; b1 = b2; b2 = t;
        }
    } else {
        auto vout = [&](int s) {
#pragma unroll
            for (int i = 0; i < 4; ++i) {
                f4 t = c[i] + __builtin_bit_cast(f4, va[0][i]);
                sts8(vw + (s & 1) * 8192 + (i >> 1) * 4096 + (i & 1) * 512, u2{pk(t[0], t[1]), pk(t[2], t[3])});
            }
        };
        xpre(b0, 1);
        vproj(0, 1);
        vout(0);
        wait_vm();
        X.dma9(sg(2), b2);
        bar();
        b0 = 36864; b1 = 73728; b2 = 0;
#pragma clang loop unroll(disable)
        for (int s = 1; s < 64; ++s) {
#pragma unroll
            for (int i = 0; i < 4; ++i) vload(s, i);
            wait_vm();
            X.dma9(min(sg(s + 2), 63), b2);
            uproj(s, 4);
            vproj(s, 1);
            vout(s);
            if (s < pub1 || s == 63) wait_vm();
            bar();
            unsigned t = b0; b0 = b1; b1 = b2; b2 = t;
        }
    }
}

DI void role_XO(const Args& a, char* lds, int T0, int s0, int s1, int tid, unsigned tg) {
    const int lane = tid & 63, wv = __builtin_amdgcn_readfirstlane(tid >> 6);
    rsrc_t ws = mk(a.ws, W_END), ro = mk(a.out, 2048 * 1152 * 2);
    const int g4 = lane >> 4, col = lane & 15;
    const unsigned wb = lane < 18 ? W_U + lane * 65536 : lane < 22 ? W_KX + (lane - 18) * 65536 : lane < 26 ? W_QX + (lane - 22) * 65536 : lane < 31 ? W_ZA + (lane - 26) * 65536 : W_PP + (lane & 1) * 65536;
    const unsigned wst = lane < 18 ? 36 * 32768 : lane < 26 ? 32 * 8192 : lane < 31 ? Z_SLOT : 32 * 2560;
    auto warm = [&](int sg) { unsigned v = ld4<SC1>(ws, wb + wst * sg); asm volatile("" :: "v"(v)); };
    const int wd = T_WD;
    if (s1 == 62 && wd) warm(wv);
    for (int s = s0; s < s1; s += 2) {
        u4 u[3][4], z[3], p[4][2], pm[2];
        if (s1 == 62 && wv == 7 && wd && s + wd < 64) warm(s + wd);
        if (wv == 0) {
            wait_slow(ws, tg, F_CPROG, 1, s < 62 ? s + 1 : 60, lane);
            if (s >= 62) wait_slow(ws, tg, fhot(a.ws) + F_UPROG, 32, 64, lane);
        }
        bar();
#pragma unroll
        for (int t = 0; t < 3; ++t)
#pragma unroll
            for (int i = 0; i < 4; ++i) u[t][i] = ld16<SC1>(ws, W_U + (unsigned)(4 * wv + i) * 1024 + lane * 16, (unsigned)(s * 36 + T0 + t) * 32768);
        rsrc_t zr = ws;
        unsigned zb = W_ZA + (unsigned)(s * 36 + T0) * 8192, zs = 8192;
        if (s >= 62) {
            if (s == 63) { zr = mkring(a.ws); zb = ZB + ((wait_rdy(ws, tg, F_RDYR, 2, lane) & 2) << 12) + T0 * 16384; zs = 16384; }
            if (wv == 0) { wait_slow(ws, tg, F_CPROG, 1, 62, lane); if (s == 62) wait_rdy(ws, tg, F_ZPROG, 64, lane); }
            bar();
        }
        const unsigned pb = W_PP + (unsigned)(s * 32 + 4 * wv) * 2560;
#pragma clang loop unroll(disable)
        for (unsigned sp = 0; sp < SPIN_MAX; ++sp) {
            cfence();
#pragma unroll
            for (int t = 0; t < 3; ++t) z[t] = ld16<SC1>(zr, (unsigned)(4 * wv) * 256 + lane * 16, zb + t * zs);
#pragma unroll
            for (int i = 0; i < 4; ++i)
#pragma unroll
                for (int rt = 0; rt < 2; ++rt) p[i][rt] = ld16<SC1>(ws, pb + lane * 16, i * 2560 + rt * 1024);
#pragma unroll
            for (int rt = 0; rt < 2; ++rt) pm[rt] = ld16<SC1>(ws, pb + g4 * 2560 + 2048 + col * 16, rt * 256);
            if (s < 63) break;
            unsigned m = max(max(mx4(z[0]), mx4(z[1])), max(mx4(z[2]), max(mx4(pm[0]), mx4(pm[1]))));
#pragma unroll
            for (int i = 0; i < 4; ++i) m = max(m, max(mx4(p[i][0]), mx4(p[i][1])));
            if (!anybad(m)) break;
        }
        char* red = lds + ((s >> 1) & 1) * 49152;
#pragma unroll
        for (int t = 0; t < 3; ++t)
#pragma unroll
            for (int rt = 0; rt < 2; ++rt) {
                f4 c = {0, 0, 0, 0};
#pragma unroll
                for (int i = 0; i < 4; ++i) c = mfma16(u[t][i], p[i][rt], c);
                c = mfma16(z[t], pm[rt], c);
                sts16(red + (wv * 6 + t * 2 + rt) * 1024 + lane * 16, __builtin_bit_cast(u4, c));
            }
        bar();
        if (wv < 6) {
            f4 c = __builtin_bit_cast(f4, lds16(red + wv * 1024 + lane * 16));
#pragma unroll
            for (int i = 1; i < 8; ++i) c += __builtin_bit_cast(f4, lds16(red + (i * 6 + wv) * 1024 + lane * 16));
            unsigned o = (unsigned)(s * 32 + (wv & 1) * 16 + col) * 2304 + (576 + 16 * (T0 + (wv >> 1)) + 4 * g4) * 2;
            st8<0>(u2{pk(c[0], c[1]), pk(c[2], c[3])}, ro, o);
        }
    }
}

DI unsigned swzG(unsigned row) { return 2u * (((row >> 1) & 1) | (((row >> 3) & 1) << 1)); }
template <int XAUX, int OAUX>
DI void gemm_item(rsrc_t xr_, unsigned xoff, rsrc_t wr, unsigned ldw, rsrc_t orr, unsigned ooff, unsigned ldo, int row0, int col0, int npass, float osc, char* lds, int tid) {
    const int lane = tid & 63, wv = __builtin_amdgcn_readfirstlane(tid >> 6);
    const int g4 = lane >> 4, col = lane & 15;
    const unsigned wsrc = (unsigned)(tid >> 3) * ldw + (tid & 7) * 16 + col0 * 2;
    const unsigned wdst = (unsigned)(tid >> 3) * 128 + 16 * ((tid & 7) ^ swzG(tid >> 3));
    bar();
    {
        u4 t[9];
#pragma unroll
        for (int i = 0; i < 9; ++i) t[i] = ld16<0>(wr, wsrc, i * 64 * ldw);
#pragma unroll
        for (int i = 0; i < 9; ++i) sts16(lds + wdst + i * 8192, t[i]);
    }
    u4 x[2][18];
    {
        unsigned o = xoff + (unsigned)(row0 + wv * 32 + col) * 1152 + g4 * 16;
#pragma unroll
        for (int j = 0; j < 18; ++j) { x[0][j] = ld16<XAUX>(xr_, o, j * 64); x[1][j] = ld16<XAUX>(xr_, o, 16 * 1152 + j * 64); }
    }
    bar();
    const unsigned q = (lane >> 2) & 3, p = lane & 3;
    unsigned ra[2], rbs[2];
#pragma unroll
    for (int u = 0; u < 2; ++u) { ra[u] = (8 * g4 + 4 * u + q) * 128 + 8 * (p & 1); rbs[u] = swzG(8 * g4 + 4 * u + q); }
    for (int ps = 0; ps < npass; ++ps) {
        const char* wb = lds + (ps & 1) * 73728;
        char* wn = lds + ((ps + 1) & 1) * 73728;
        const bool more = ps + 1 < npass;
        f4 c[2][4] = {};
        u4 t[3];
#pragma unroll
        for (int j = 0; j < 18; ++j) {
            if (j % 6 == 0 && more) {
#pragma unroll
                for (int i = 0; i < 3; ++i) t[i] = ld16<0>(wr, wsrc + (ps + 1) * 128, ((j / 6) * 3 + i) * 64 * ldw);
            }
#pragma unroll
            for (int ct = 0; ct < 4; ++ct) {
                u2 w0 = ldtr(wb + j * 4096 + ra[0] + 16 * ((2 * ct + (p >> 1)) ^ rbs[0]));
                u2 w1 = ldtr(wb + j * 4096 + ra[1] + 16 * ((2 * ct + (p >> 1)) ^ rbs[1]));
                u4 wf = {w0[0], w0[1], w1[0], w1[1]};
                c[0][ct] = mfma16(wf, x[0][j], c[0][ct]); c[1][ct] = mfma16(wf, x[1][j], c[1][ct]);
            }
            if (j % 6 == 5 && more) {
#pragma unroll
                for (int i = 0; i < 3; ++i) sts16(wn + wdst + ((j / 6) * 3 + i) * 8192, t[i]);
            }
        }
#pragma unroll
        for (int rt = 0; rt < 2; ++rt)
#pragma unroll
            for (int cp = 0; cp < 2; ++cp) {
                c[rt][2 * cp] *= osc; c[rt][2 * cp + 1] *= osc;
                u2 X = {pk(c[rt][2 * cp][0], c[rt][2 * cp][1]), pk(c[rt][2 * cp][2], c[rt][2 * cp][3])};
                u2 Y = {pk(c[rt][2 * cp + 1][0], c[rt][2 * cp + 1][1]), pk(c[rt][2 * cp + 1][2], c[rt][2 * cp + 1][3])};
                u4 o = widen16(X, Y);
                unsigned oo = ooff + (unsigned)(row0 + wv * 32 + rt * 16 + col) * ldo + (col0 + ps * 64 + cp * 32 + (g4 & 1) * 16 + (g4 >> 1) * 8) * 2;
                st16<OAUX>(o, orr, oo);
            }
        bar();
    }
}
DI void qkv_item(const Args& a, char* lds, int it, int tid, unsigned tg) {
    rsrc_t ws = mk(a.ws, W_END);
    gemm_item<0, SC1>(mk(a.ax, 2048 * 576 * 2), 0, mk(a.awqkv, 576 * 1728 * 2), 3456, ws, W_QKV, 3456, (it / 9) * 256, (it % 9) * 192, 3,
                      it % 9 < 3 ? 0.23570226039551584f * 1.4426950408889634f : 1.0f, lds, tid);
    wait_vm(); bar();
    if (tid == 0) setf(ws, tg, F_G1 + it, 1);
}
DI void oproj_item(const Args& a, char* lds, int rb, int pass, int tid, unsigned tg) {
    rsrc_t ws = mk(a.ws, W_END);
    bool dead = false; unsigned have = 0;
    wait_prog(ws, tg, F_APROG + (rb >> 2) * 32, 32, ((rb >> 1) & 1) + 1, have, dead, tid & 63);
    gemm_item<SC1, 0>(ws, W_OA, mk(a.awo, 576 * 576 * 2), 1152, mk(a.out, 2048 * 1152 * 2), 0, 2304, rb * 256, pass * 64, 1, 1.0f, lds, tid);
}

DI void attn_role(const Args& a, char* lds, int h, int hf, int tid, unsigned tg) {
    const int lane = tid & 63, wv = __builtin_amdgcn_readfirstlane(tid >> 6);
    rsrc_t ws = mk(a.ws, W_END);
    bool dead = false; unsigned h1 = 0, h2 = 0;
    wait_prog(ws, tg, F_G1, 64, 1, h1, dead, lane);
    wait_prog(ws, tg, F_G1 + 64, 8, 1, h2, dead, lane);
    char* km = lds; char* ke = lds + 65536; char* vi = lds + 65536 + 8192;
    unsigned d0 = rnow();
    bar();
#pragma unroll
    for (int i = 0; i < 4; ++i) {
        int key = i * 512 + tid;
        unsigned o = W_QKV + (unsigned)key * 3456 + 1152 + h * 36;
        u4 k0 = ld16<SC1>(ws, o), k1 = ld16<SC1>(ws, o + 16); unsigned k2 = ld4<SC1>(ws, o + 32);
        u4 v0 = ld16<SC1>(ws, o + 1152), v1 = ld16<SC1>(ws, o + 1168); unsigned v2 = ld4<SC1>(ws, o + 1184);
        sts16(km + (key >> 5) * 1024 + (key & 31) * 16, k0);
        sts16(km + (key >> 5) * 1024 + 512 + (key & 31) * 16, k1);
        sts4(ke + (key >> 6) * 256 + (key & 31) * 8 + ((key >> 5) & 1) * 4, k2);
        char* vp = vi + key * 40;
        sts8(vp, u2{v0[0], v0[1]}); sts8(vp + 8, u2{v0[2], v0[3]}); sts8(vp + 16, u2{v1[0], v1[1]}); sts8(vp + 24, u2{v1[2], v1[3]});
        sts8(vp + 32, u2{v2, 0x00003F80u});
    }
    bar();
    const int hh = lane >> 5, c32 = lane & 31;
    const char* kp = km + lane * 16;
    const char* kep = ke + c32 * 8;
    const char* vp = vi + (4 * hh + ((lane >> 2) & 3)) * 40 + (((lane >> 4) & 1) ? 32 : 8 * (lane & 3));
    const f16v z16 = {};
    auto vfrag = [&](int t, int s2) { const char* v = vp + (t * 32 + s2 * 16) * 40; u2 x = ldtr(v), y = ldtr(v + 320); return u4{x[0], x[1], y[0], y[1]}; };
    auto pfrag = [&](const f16v& sc, int s2, float m) {
        u4 pf;
#pragma unroll
        for (int i = 0; i < 4; ++i) pf[i] = pk(ex2(sc[8 * s2 + 2 * i] - m), ex2(sc[8 * s2 + 2 * i + 1] - m));
        return pf;
    };
#pragma clang loop unroll(disable)
    for (int ps = 0; ps < 2; ++ps) {
        const int row = hf * 1024 + ps * 512 + wv * 64 + c32;
        u4 q0[2]; unsigned qe[2]; u2 qa[2], qb[2];
        f16v o[2], sa[2], sb[2];
#pragma unroll
        for (int i = 0; i < 2; ++i) {
            unsigned qo = W_QKV + (unsigned)(row + 32 * i) * 3456 + h * 36;
            q0[i] = ld16<SC1>(ws, qo + hh * 16);
            qe[i] = ld4<SC1>(ws, qo + 32);
            if (hh) qe[i] = 0;
        }
        u2 e = lds8(kep);
#pragma unroll
        for (int i = 0; i < 2; ++i) {
            f16v c = mfma32k8(e, u2{qe[i], 0}, mfma32(lds16(kp), q0[i], z16)), d = mfma32k8(e, u2{0, qe[i]}, mfma32(lds16(kp + 1024), q0[i], z16));
            float mx = fmx(c[0], d[0]);
#pragma unroll
            for (int k = 1; k < 16; ++k) mx = fmx(mx, fmx(c[k], d[k]));
            unsigned nm = pk(-xmax32(mx), 0.f);
            qa[i] = u2{hh ? nm : qe[i], 0}; qb[i] = u2{hh ? nm : 0, qe[i]};
            o[i] = z16;
        }
        if (hh) e[0] = 0x3F80u;
        {
            u4 kf = lds16(kp);
#pragma unroll
            for (int i = 0; i < 2; ++i) sa[i] = mfma32k8(e, qa[i], mfma32(kf, q0[i], z16));
        }
        unsigned d1 = rnow(), d2 = (unsigned)__builtin_amdgcn_s_memtime();
        {
            lp kq = (lp)(kp + 1024), eq = (lp)(kep + 256), vq = (lp)vp;
            u4 kf = lds16(kq), v0 = vfrag(0, 0), v1 = vfrag(0, 1), vl = {0, 0, 0, 0}, pl = vl;
            auto half = [&](const f16v (&si)[2], f16v (&so)[2], const u2 (&qx)[2], bool odd) {
                float x[8]; u4 p;
                auto exa = [&](int i, int s2) { for (int k = 0; k < 4; ++k) x[k] = ex2(si[i][8 * s2 + k]); SB(); };
                auto exb = [&](int i, int s2) { for (int k = 4; k < 8; ++k) x[k] = ex2(si[i][8 * s2 + k]); p = u4{pk(x[0], x[1]), pk(x[2], x[3]), pk(x[4], x[5]), pk(x[6], x[7])}; SB(); };
                so[0] = mfma32(kf, q0[0], z16); SB(); exa(0, 0);
                o[1] = mfma32(vl, pl, o[1]); SB(); exb(0, 0);
                so[1] = mfma32(kf, q0[1], z16); kf = lds16(kq + (odd ? 2048 : 1024)); SB(); pl = p; exa(0, 1);
                o[0] = mfma32(v0, pl, o[0]); SB(); exb(0, 1);
                so[0] = mfma32k8(e, qx[0], so[0]); SB(); pl = p; exa(1, 0);
                o[0] = mfma32(v1, pl, o[0]); SB(); exb(1, 0);
                so[1] = mfma32k8(e, qx[1], so[1]); if (!odd) e = lds8(eq); SB(); pl = p; exa(1, 1);
                o[1] = mfma32(v0, pl, o[1]); vl = v1;
                lp v = vq + (odd ? 2560 : 1280);
                u2 a0 = ldtr(v), a1 = ldtr(v + 320), a2 = ldtr(v + 640), a3 = ldtr(v + 960);
                SB(); exb(1, 1);
                v0 = u4{a0[0], a0[1], a1[0], a1[1]}; v1 = u4{a2[0], a2[1], a3[0], a3[1]};
                if (!odd && hh) e[0] = 0x3F80u;
                pl = p;
            };
#pragma clang loop unroll(disable)
            for (int t = 0; t < 32; ++t) {
                asm volatile("" : "+v"(kq), "+v"(eq), "+v"(vq));
                half(sa, sb, qb, false);
                half(sb, sa, qa, true);
                kq += 2048; eq += 256; vq += 2560;
            }
            o[1] = mfma32(vl, pl, o[1]);
        }
        asm volatile("" :: "v"(o[0]), "v"(o[1]));
#pragma unroll
        for (int i = 0; i < 2; ++i) {
            bool ok = o[i][10] < 1.0e30f;
#pragma unroll
            for (int k = 0; k < 10; ++k) ok = ok && __builtin_fabsf(o[i][k]) < 3.0e38f;
            if (UNL(!wall(ok))) {
                f16v oo = z16;
                float m = -INFINITY;
#pragma clang loop unroll(disable)
                for (int t = 0; t < 64; ++t) {
                    f16v c = mfma32k8(lds8(kep + (t >> 1) * 256), (t & 1) ? u2{0, qe[i]} : u2{qe[i], 0}, mfma32(lds16(kp + t * 1024), q0[i], z16));
                    float mx = m;
#pragma unroll
                    for (int k = 0; k < 16; ++k) mx = fmx(mx, c[k]);
                    mx = xmax32(mx);
                    float al = ex2(m - mx);
                    m = mx;
#pragma unroll
                    for (int k = 0; k < 12; ++k) oo[k] *= al;
                    oo = mfma32(vfrag(t, 0), pfrag(c, 0, m), oo); oo = mfma32(vfrag(t, 1), pfrag(c, 1, m), oo);
                }
                o[i] = oo;
            }
            float r = 1.0f / o[i][10];
            unsigned oo = W_OA + (unsigned)(row + 32 * i) * 1152 + h * 36 + hh * 8;
            st8<SC1>(u2{pk(o[i][0] * r, o[i][1] * r), pk(o[i][2] * r, o[i][3] * r)}, ws, oo);
            st8<SC1>(u2{pk(o[i][4] * r, o[i][5] * r), pk(o[i][6] * r, o[i][7] * r)}, ws, oo + 16);
            if (hh == 0) st4<SC1>(pk(o[i][8] * r, o[i][9] * r), ws, oo + 32);
        }
        wait_vm(); bar();
        if (tid == 0) setf(ws, tg, F_APROG + hf * 32 + h, ps + 1);
    }
}

__global__ void __launch_bounds__(512, 2) mega_kernel(Args a) {
    extern __shared__ __attribute__((aligned(16))) char lds[];
    const int tid = threadIdx.x, x = blockIdx.x & 7, sl = blockIdx.x >> 3;
    rsrc_t ws = mk(a.ws, W_END);
    const unsigned ev = ld4<SC1>(ws, W_EPOCH);
    cfence();
    if (sl < 16 || (x < 4 && sl < 25)) {
        if (x < 4) {
            if (sl < 8) role_A(a, lds, x * 8 + sl, x, tid, ev);
            else if (sl < 16) role_V(a, lds, x * 8 + sl - 8, x, tid, ev);
            else role_R(a, lds, x * 9 + sl - 16, x, tid, ev);
        } else {
            if (sl < 8) {
                const int h = (x - 4) * 8 + sl;
                const unsigned tg = role_QK(a, lds, h, tid, ev);
                if (h < 24) role_XO(a, lds, 3 * (h % 12), 62 + h / 12, 64, tid, tg);
            } else role_U(a, lds, (x - 4) * 8 + sl - 8, tid, ev);
        }
        return;
    }
    const unsigned tg = take_tag(ev);
    bar();
    arrive(ws, tg, tid);
    int qi = -1;
    if (x >= 4) qi = (x - 4) * 16 + sl - 16;
    if (x < 4 && sl == 31) qi = 64 + x;
    if (x < 4 && sl == 30) qi = 68 + x;
    if (qi >= 0) {
        for (int i = T_PA; i > 0; --i) __builtin_amdgcn_s_sleep(32);
        qkv_item(a, lds, qi, tid, tg);
    }
    if (blockIdx.x == 248 && tid < 64) {
#pragma clang loop unroll(disable)
        for (unsigned sp = 0; sp < SPIN_MAX; ++sp) {
            cfence();
            u4 v = ld16<SC1>(ws, W_ARR + tid * 16);
            if (wall(v[0] == tg && v[1] == tg && v[2] == tg && v[3] == tg)) break;
            __builtin_amdgcn_s_sleep(16);
        }
        cfence();
        if (tid == 0) st4<SC1>(tg, ws, W_EPOCH);
    }
    if (x < 4 && sl < 31) role_XO(a, lds, x * 9 + 3 * ((sl - 25) >> 1), (sl - 25) & 1, 62, tid, tg);
    if (qi >= 0 && qi < 64) attn_role(a, lds, qi >> 1, qi & 1, tid, tg);
    if (qi >= 0 && qi < 68) {
        int n = qi < 64 ? 1 : 2;
        for (int i = 0; i < n; ++i) oproj_item(a, lds, qi < 64 ? qi >> 3 : (qi - 64) + 4 * i, qi < 64 ? qi & 7 : 8, tid, tg);
    }
}

extern "C" void kernel_launch(void* const* d_in, const int* in_sizes, int n_in, void* d_out, int out_size, void* d_ws, size_t ws_size,
                              hipStream_t stream) {
    (void)in_sizes; (void)n_in; (void)out_size; (void)ws_size;
    Args a;
    a.ax = d_in[0]; a.awqkv = d_in[1]; a.awo = d_in[2]; a.bx = d_in[3]; a.mem0 = d_in[4];
    a.wq = d_in[5]; a.wk = d_in[6]; a.wv = d_in[7]; a.wo = d_in[8]; a.out = d_out; a.ws = d_ws;
    (void)hipFuncSetAttribute((const void*)mega_kernel, hipFuncAttributeMaxDynamicSharedMemorySize, LDS_BYTES);
    mega_kernel<<<256, 512, LDS_BYTES, stream>>>(a);
}
```
